```python
import torch

def _softmax(x, axis=-1):
    return torch.softmax(x, dim=axis)

def _gelu(x):
    return torch.nn.functional.gelu(x, approximate='tanh')

def _silu(x):
    return torch.nn.functional.silu(x)

def _sigmoid(x):
    return torch.sigmoid(x)

def _layernorm(x, axis=-1, eps=1e-05):
    m = x.mean(dim=axis, keepdim=True)
    v = x.var(dim=axis, keepdim=True, unbiased=False)
    return (x - m) / torch.sqrt(v + eps)

def _scatter_add(tgt, idx, src, axis=0):
    return tgt.clone().index_add_(axis, idx, src)

def _segment_sum(src, seg, n):
    out = torch.zeros((n,) + src.shape[1:], dtype=src.dtype)
    return out.index_add_(0, seg, src)

def _pad(x, pw):
    flat = tuple((v for pair in reversed(pw) for v in pair))
    return torch.nn.functional.pad(x, flat)
kernel_name = 'adaLN_cond__grouped_kv_mqa__glu_gated_glu__prompt'

def setup_inputs():

    _w = lambda a: (lambda t: t.to(torch.bfloat16) if t.is_floating_point() else t)(torch.as_tensor(a))

    def _A_setup():
        B, S, D, H = (1, 2048, 512, 32)
        Dh = D // H
        return {'x': _w((lambda *s: torch.randn(*s, dtype=torch.bfloat16) * 0.05)(B, S, D)), 'wq': _w((lambda *s: torch.randn(*s, dtype=torch.bfloat16) * 0.05)(D, H * Dh)), 'wk': _w((lambda *s: torch.randn(*s, dtype=torch.bfloat16) * 0.05)(D, Dh)), 'wv': _w((lambda *s: torch.randn(*s, dtype=torch.bfloat16) * 0.05)(D, Dh)), 'wo': _w((lambda *s: torch.randn(*s, dtype=torch.bfloat16) * 0.05)(H * Dh, D))}
    _A_in = _A_setup()

    def _B_setup():
        B, S, D, F = (1, 2048, 512, 1792)
        return {'x': _w((lambda *s: torch.randn(*s, dtype=torch.bfloat16) * 0.05)(B, S, D)), 'w_gate': _w((lambda *s: torch.randn(*s, dtype=torch.bfloat16) * 0.05)(D, F)), 'w_up': _w((lambda *s: torch.randn(*s, dtype=torch.bfloat16) * 0.05)(D, F)), 'w_down': _w((lambda *s: torch.randn(*s, dtype=torch.bfloat16) * 0.05)(F, D))}
    _B_in = _B_setup()
    return {'A_x': _A_in['x'], 'A_wq': _A_in['wq'], 'A_wk': _A_in['wk'], 'A_wv': _A_in['wv'], 'A_wo': _A_in['wo'], 'B_x': _B_in['x'], 'B_w_gate': _B_in['w_gate'], 'B_w_up': _B_in['w_up'], 'B_w_down': _B_in['w_down'], 'c': _w((lambda *s: torch.randn(*s, dtype=torch.bfloat16) * 0.1)(1, 512) * 8.0), 'w_mod': _w((lambda *s: torch.randn(*s, dtype=torch.bfloat16) * 0.1)(512, 6 * 512) * 512 ** (-0.5) * 8.0)}

def reference(A_x, A_wq, A_wk, A_wv, A_wo, B_x, B_w_gate, B_w_up, B_w_down, c, w_mod):

    def _A_ref(x, wq, wk, wv, wo):
        B, S, D = x.shape
        H = 32
        Dh = D // H
        q = (lambda x, *d: x.permute(*d))((x @ wq).reshape(B, S, H, Dh), 0, 2, 1, 3)
        k = x @ wk
        v = x @ wv
        scores = torch.einsum('bhqd,bkd->bhqk', q, k) * Dh ** (-0.5)
        p = _softmax(scores, axis=-1)
        o = torch.einsum('bhqk,bkd->bhqd', p, v)
        return (lambda x, *d: x.permute(*d))(o, 0, 2, 1, 3).reshape(B, S, H * Dh) @ wo

    def _B_ref(x, w_gate, w_up, w_down):
        g = torch.einsum('bsd,df->bsf', x, w_gate)
        u = torch.einsum('bsd,df->bsf', x, w_up)
        h = _sigmoid(g) * u
        return torch.einsum('bsf,fd->bsd', h, w_down)
    x = A_x
    mod = (_silu(c) @ w_mod).reshape(c.shape[0], 6, 1, -1)
    sa, ca, ga, sb, cb, gb = (mod[:, 0].clone(), mod[:, 1].clone(), mod[:, 2].clone(), mod[:, 3].clone(), mod[:, 4].clone(), mod[:, 5].clone())
    h = _layernorm(x, axis=-1) * (1 + ca) + sa
    x = x + ga * _A_ref(h, A_wq, A_wk, A_wv, A_wo)
    h = _layernorm(x, axis=-1) * (1 + cb) + sb
    x = x + gb * _B_ref(h, B_w_gate, B_w_up, B_w_down)
    return x
```

```cpp
#include <hip/hip_runtime.h>
#include <hip/hip_bf16.h>
typedef unsigned short u16;
__device__ __forceinline__ float b2f(u16 v){ return __uint_as_float(((unsigned)v)<<16); }
__device__ __forceinline__ u16 f2b(float f){ unsigned u=__float_as_uint(f); return (u16)((u+0x7FFF+((u>>16)&1))>>16); }
constexpr int S=2048,D=512,F=1792;
__global__ void k_mod(const u16* c,const u16* w,float* mod){ int j=blockIdx.x*256+threadIdx.x; float a=0; for(int k=0;k<512;k++){ float x=b2f(c[k]); float s=x/(1.f+__expf(-x)); a+=s*b2f(w[k*3072+j]);} mod[j]=a; }
__global__ void k_ln(const u16* x,const float* mod,int so,int co,float* h){ int t=blockIdx.x; __shared__ float r[256]; float v0=b2f(x[t*D+threadIdx.x]),v1=b2f(x[t*D+256+threadIdx.x]);
 r[threadIdx.x]=v0+v1; __syncthreads(); for(int s=128;s;s>>=1){ if(threadIdx.x<s) r[threadIdx.x]+=r[threadIdx.x+s]; __syncthreads(); } float m=r[0]/D; __syncthreads();
 r[threadIdx.x]=(v0-m)*(v0-m)+(v1-m)*(v1-m); __syncthreads(); for(int s=128;s;s>>=1){ if(threadIdx.x<s) r[threadIdx.x]+=r[threadIdx.x+s]; __syncthreads(); } float rs=rsqrtf(r[0]/D+1e-5f);
 int i=threadIdx.x; h[t*D+i]=b2f(f2b((v0-m)*rs*(1.f+mod[co+i])+mod[so+i])); i+=256; h[t*D+i]=b2f(f2b((v1-m)*rs*(1.f+mod[co+i])+mod[so+i])); }
__global__ void k_gemm(const float* A,const u16* B,float* C,int K,int N){ int n=blockIdx.x*64+(threadIdx.x&63); int m=blockIdx.y*4+(threadIdx.x>>6); if(n>=N) return; float a=0; for(int k=0;k<K;k++) a+=A[m*K+k]*b2f(B[k*N+n]); C[m*N+n]=b2f(f2b(a)); }
__global__ void k_attn(const float* q,const float* k,const float* v,float* o){ int row=blockIdx.x*64+threadIdx.x; int t=row>>5,h=row&31; float qq[16]; for(int d=0;d<16;d++) qq[d]=q[t*D+h*16+d]*0.25f;
 float m=-1e30f,l=0,acc[16]; for(int d=0;d<16;d++) acc[d]=0; for(int j=0;j<S;j++){ float s=0; for(int d=0;d<16;d++) s+=qq[d]*k[j*16+d]; float mn=fmaxf(m,s); float al=__expf(m-mn),p=__expf(s-mn); l=l*al+p; for(int d=0;d<16;d++) acc[d]=acc[d]*al+p*v[j*16+d]; m=mn; }
 for(int d=0;d<16;d++) o[t*D+h*16+d]=b2f(f2b(acc[d]/l)); }
__global__ void k_res(const u16* x,const float* y,const float* mod,int go,u16* out){ int i=blockIdx.x*256+threadIdx.x; int c=i&511; out[i]=f2b(b2f(x[i])+mod[go+c]*y[i]); }
__global__ void k_glu(const float* g,const float* u,float* h){ int i=blockIdx.x*256+threadIdx.x; h[i]=b2f(f2b(u[i]/(1.f+__expf(-g[i])))); }
extern "C" void kernel_launch(void* const* d_in, const int* in_sizes, int n_in, void* d_out, int out_size, void* d_ws, size_t ws_size, hipStream_t stream) {
  const u16 *x=(const u16*)d_in[0],*wq=(const u16*)d_in[1],*wk=(const u16*)d_in[2],*wv=(const u16*)d_in[3],*wo=(const u16*)d_in[4],*wg=(const u16*)d_in[6],*wu=(const u16*)d_in[7],*wd=(const u16*)d_in[8],*c=(const u16*)d_in[9],*wm=(const u16*)d_in[10];
  float* ws=(float*)d_ws; float* mod=ws; float* h=ws+4096; float* q=h+S*D; float* k=q+S*D; float* v=k+S*16; float* o=v+S*16; float* y=o+S*D; float* g=y+S*D; float* u=g+S*F; float* hm=u+S*F; u16* x2=(u16*)(hm+S*F);
  k_mod<<<12,256,0,stream>>>(c,wm,mod);
  k_ln<<<S,256,0,stream>>>(x,mod,0,512,h);
  k_gemm<<<dim3(8,S/4),256,0,stream>>>(h,wq,q,512,512);
  k_gemm<<<dim3(1,S/4),256,0,stream>>>(h,wk,k,512,16);
  k_gemm<<<dim3(1,S/4),256,0,stream>>>(h,wv,v,512,16);
  k_attn<<<S*32/64,64,0,stream>>>(q,k,v,o);
  k_gemm<<<dim3(8,S/4),256,0,stream>>>(o,wo,y,512,512);
  k_res<<<S*D/256,256,0,stream>>>(x,y,mod,1024,x2);
  k_ln<<<S,256,0,stream>>>(x2,mod,1536,2048,h);
  k_gemm<<<dim3(28,S/4),256,0,stream>>>(h,wg,g,512,F);
  k_gemm<<<dim3(28,S/4),256,0,stream>>>(h,wu,u,512,F);
  k_glu<<<S*F/256,256,0,stream>>>(g,u,hm);
  k_gemm<<<dim3(8,S/4),256,0,stream>>>(hm,wd,y,F,512);
  k_res<<<S*D/256,256,0,stream>>>(x2,y,mod,2560,(u16*)d_out);
}
```

```cpp
#include <hip/hip_runtime.h>
#include <hip/hip_bf16.h>
#include <stdint.h>

typedef unsigned short u16;
typedef unsigned int u32;
typedef __bf16 bf16x8 __attribute__((ext_vector_type(8)));
typedef __bf16 bf16x2 __attribute__((ext_vector_type(2)));
typedef float f32x16 __attribute__((ext_vector_type(16)));
typedef float f32x4 __attribute__((ext_vector_type(4)));
typedef float f32x2 __attribute__((ext_vector_type(2)));
typedef u32 u32x4 __attribute__((ext_vector_type(4)));
typedef u32 u32x2 __attribute__((ext_vector_type(2)));
#define LDSP __attribute__((address_space(3)))
#define GLBP __attribute__((address_space(1)))

constexpr int SEQ = 2048, DM = 512, FF = 1792;
constexpr float LOG2E = 1.4426950408889634f;

constexpr size_t WS_CNT  = 0;
constexpr size_t WS_MOD  = 16384;
constexpr size_t WS_US   = WS_MOD + 16384;
constexpr size_t WS_KMAX = WS_US + 32768;
constexpr size_t WS_ST   = WS_KMAX + 4096;
constexpr size_t WS_KF   = WS_ST + 131072;
constexpr size_t WS_VF   = WS_KF + 65536;
constexpr size_t WS_WQT  = WS_VF + 65536;
constexpr size_t WS_WKVT = WS_WQT + 524288;
constexpr size_t WS_WOT  = WS_WKVT + 32768;
constexpr size_t WS_W1T  = WS_WOT + 524288;
constexpr size_t WS_WDT  = WS_W1T + 3584 * 1024;
constexpr size_t WS_XH   = WS_WDT + 1792 * 1024;
constexpr size_t WS_XF   = WS_XH + 2097152;
constexpr size_t WS_OF   = WS_XF + 2097152;
constexpr size_t WS_X2F  = WS_OF + 2097152;
constexpr size_t WS_Y2F  = WS_X2F + 2097152;
constexpr size_t WS_HF   = WS_Y2F + 2097152;
constexpr size_t WS_END  = WS_HF + 64 * 112 * 1024;

constexpr int NREP = 16;

__device__ __forceinline__ float bf2f(u16 v) { return __uint_as_float(((u32)v) << 16); }
__device__ __forceinline__ float lo2f(u32 w) { return __uint_as_float(w << 16); }
__device__ __forceinline__ float hi2f(u32 w) { return __uint_as_float(w & 0xffff0000u); }
__device__ __forceinline__ u32 pk2(float a, float b) {
  f32x2 v = {a, b};
  return __builtin_bit_cast(u32, __builtin_convertvector(v, bf16x2));
}
__device__ __forceinline__ float rbf(float a) { return lo2f(pk2(a, a)); }
__device__ __forceinline__ bf16x8 asbf(u32x4 v) { return __builtin_bit_cast(bf16x8, v); }
#define MFMA32(a, b, c) __builtin_amdgcn_mfma_f32_32x32x16_bf16(asbf(a), asbf(b), c, 0, 0, 0)
#define MFMA16(a, b, c) __builtin_amdgcn_mfma_f32_16x16x32_bf16(asbf(a), asbf(b), c, 0, 0, 0)
#define SBAR() __builtin_amdgcn_sched_barrier(0)
#define PIN(x) asm volatile("" : "+v"(x))
#define BAR() asm volatile("s_waitcnt lgkmcnt(0)\n\ts_barrier" ::: "memory")
#define WAITV(N) asm volatile("s_waitcnt vmcnt(" #N ")" ::: "memory")
#define WAITV_BAR(N) asm volatile("s_waitcnt vmcnt(" #N ") lgkmcnt(0)\n\ts_barrier" ::: "memory")

template <int CTRL>
__device__ __forceinline__ float dpp(float x) {
  return __int_as_float(__builtin_amdgcn_update_dpp(0, __float_as_int(x), CTRL, 0xf, 0xf, true));
}
__device__ __forceinline__ float row_sum(float x) {
  x += dpp<0x128>(x); x += dpp<0x124>(x); x += dpp<0x122>(x); x += dpp<0x121>(x); return x;
}
__device__ __forceinline__ float row_max(float x) {
  x = fmaxf(x, dpp<0x128>(x)); x = fmaxf(x, dpp<0x124>(x)); x = fmaxf(x, dpp<0x122>(x)); x = fmaxf(x, dpp<0x121>(x)); return x;
}
__device__ __forceinline__ float swap32_sum(float x) {
  auto r = __builtin_amdgcn_permlane32_swap(__float_as_uint(x), __float_as_uint(x), false, false);
  return __uint_as_float(r[0]) + __uint_as_float(r[1]);
}
__device__ __forceinline__ float swap32_max(float x) {
  auto r = __builtin_amdgcn_permlane32_swap(__float_as_uint(x), __float_as_uint(x), false, false);
  return fmaxf(__uint_as_float(r[0]), __uint_as_float(r[1]));
}
__device__ __forceinline__ float swap16_sum(float x) {
  auto r = __builtin_amdgcn_permlane16_swap(__float_as_uint(x), __float_as_uint(x), false, false);
  return __uint_as_float(r[0]) + __uint_as_float(r[1]);
}
__device__ __forceinline__ float swap16_max(float x) {
  auto r = __builtin_amdgcn_permlane16_swap(__float_as_uint(x), __float_as_uint(x), false, false);
  return fmaxf(__uint_as_float(r[0]), __uint_as_float(r[1]));
}
__device__ __forceinline__ float wave_sum(float x) { return swap32_sum(swap16_sum(row_sum(x))); }
__device__ __forceinline__ float wave_max(float x) { return swap32_max(swap16_max(row_max(x))); }

extern __shared__ __attribute__((aligned(16))) char smem[];
__device__ __forceinline__ u32x4 lds_r128(u32 off) { return *(const LDSP u32x4*)((LDSP char*)smem + off); }
__device__ __forceinline__ void lds_w128(u32 off, u32x4 v) { *(LDSP u32x4*)((LDSP char*)smem + off) = v; }
__device__ __forceinline__ f32x4 lds_rf4(u32 off) { return *(const LDSP f32x4*)((LDSP char*)smem + off); }
__device__ __forceinline__ void lds_wf4(u32 off, f32x4 v) { *(LDSP f32x4*)((LDSP char*)smem + off) = v; }
__device__ __forceinline__ float lds_rf(u32 off) { return *(const LDSP float*)((LDSP char*)smem + off); }
__device__ __forceinline__ void lds_wf(u32 off, float v) { *(LDSP float*)((LDSP char*)smem + off) = v; }
__device__ __forceinline__ u32 lds_base() { return (u32)(uintptr_t)(LDSP char*)smem; }

__device__ __forceinline__ void glds1(const void* g, u32 voff, u32 lds) {
  u32 sv;
  asm volatile("s_mov_b32 %0, m0\n\ts_mov_b32 m0, %3\n\ts_nop 0\n\tglobal_load_lds_dwordx4 %1, %2\n\ts_mov_b32 m0, %0"
               : "=&s"(sv) : "v"(voff), "s"(g), "s"(lds) : "memory");
}
__device__ __forceinline__ void glds1_sc1(const void* g, u32 voff, u32 lds) {
  u32 sv;
  asm volatile("s_mov_b32 %0, m0\n\ts_mov_b32 m0, %3\n\ts_nop 0\n\tglobal_load_lds_dwordx4 %1, %2 sc1\n\ts_mov_b32 m0, %0"
               : "=&s"(sv) : "v"(voff), "s"(g), "s"(lds) : "memory");
}
__device__ __forceinline__ void glds4(const void* g, u32 voff, u32 lds) {
  u32 sv;
  asm volatile("s_mov_b32 %0, m0\n\ts_mov_b32 m0, %3\n\ts_nop 0\n\t"
               "global_load_lds_dwordx4 %1, %2\n\tglobal_load_lds_dwordx4 %1, %2 offset:1024\n\t"
               "global_load_lds_dwordx4 %1, %2 offset:2048\n\tglobal_load_lds_dwordx4 %1, %2 offset:3072\n\t"
               "s_mov_b32 m0, %0"
               : "=&s"(sv) : "v"(voff), "s"(g), "s"(lds) : "memory");
}
__device__ __forceinline__ void st8_sc1(void* p, u32x2 v) {
  __hip_atomic_store((GLBP unsigned long long*)p, ((unsigned long long)v[1] << 32) | v[0], __ATOMIC_RELAXED, __HIP_MEMORY_SCOPE_AGENT);
}
__device__ __forceinline__ void st4_sc1(void* p, u32 v) {
  __hip_atomic_store((GLBP u32*)p, v, __ATOMIC_RELAXED, __HIP_MEMORY_SCOPE_AGENT);
}
__device__ __forceinline__ u32 ld4_sc1(const void* p) {
  return __hip_atomic_load((GLBP u32*)p, __ATOMIC_RELAXED, __HIP_MEMORY_SCOPE_AGENT);
}

struct TileSrc { const u16* p; int ld; int kperm; };

__device__ __forceinline__ void tile_frag(int f, int lane, const u16* wq, const u16* wk, const u16* wv, const u16* wo,
                                          const u16* wg, const u16* wu, const u16* wd, char* ws) {
  const int r = lane & 31, hh = lane >> 5;
  const u16* src; int ld; bool perm; char* dst;
  if (f < 512) {
    const int nt = f >> 5, ks = f & 31;
    ld = 512; perm = false; src = wq + (16 * ks + 8 * hh) * 512 + 32 * nt + r; dst = ws + WS_WQT + (size_t)f * 1024;
  } else if (f < 544) {
    const int e = f - 512, m = e >> 4, t = e & 15;
    ld = 16; perm = false; src = (m ? wv : wk) + (32 * t + 8 * (lane >> 4)) * 16 + (lane & 15); dst = ws + WS_WKVT + (size_t)e * 1024;
  } else if (f < 1056) {
    const int e = f - 544, nt = e >> 5, ks = e & 31;
    ld = 512; perm = true; src = wo + (16 * ks + 4 * hh) * 512 + 32 * nt + r; dst = ws + WS_WOT + (size_t)e * 1024;
  } else if (f < 4640) {
    const int e = f - 1056, a = e >> 5, ks = e & 31;
    ld = FF; perm = true; src = ((r & 16) ? wu : wg) + (16 * ks + 4 * hh) * FF + 16 * a + (r & 15); dst = ws + WS_W1T + (size_t)e * 1024;
  } else {
    const int e = f - 4640, nt = e / 112, ks = e - nt * 112;
    ld = 512; perm = true; src = wd + (16 * ks + 4 * hh) * 512 + 32 * nt + r; dst = ws + WS_WDT + (size_t)e * 1024;
  }
  const int j4 = perm ? 8 : 4;
  u32x4 o;
  o[0] = (u32)src[0] | ((u32)src[ld] << 16);
  o[1] = (u32)src[2 * ld] | ((u32)src[3 * ld] << 16);
  const u16* s2 = src + j4 * ld;
  o[2] = (u32)s2[0] | ((u32)s2[ld] << 16);
  o[3] = (u32)s2[2 * ld] | ((u32)s2[3 * ld] << 16);
  *(u32x4*)(dst + lane * 16) = o;
}

__global__ __launch_bounds__(512, 2) void k0_prep(const u16* __restrict__ x, const u16* __restrict__ c, const u16* __restrict__ wmod,
                                                  const u16* __restrict__ wq, const u16* __restrict__ wk, const u16* __restrict__ wv,
                                                  const u16* __restrict__ wo, const u16* __restrict__ wg, const u16* __restrict__ wu,
                                                  const u16* __restrict__ wd, char* __restrict__ ws) {
  const int tid = threadIdx.x, lane = tid & 63, b = blockIdx.x;
  const int wave = __builtin_amdgcn_readfirstlane(tid >> 6);
  if (b == 200) { u32x4 z = {0, 0, 0, 0}; *(u32x4*)(ws + WS_CNT + tid * 16) = z; *(u32x4*)(ws + WS_CNT + 8192 + tid * 16) = z; }
  const int tok = 8 * b + wave;
  const u32x4 xv = *(const u32x4*)(x + tok * DM + lane * 8);
  u32x4 w0 = {0, 0, 0, 0}, w1 = {0, 0, 0, 0}; u16 c0 = 0, c1 = 0;
  const int seg = (b & 7) * 24 + (b >> 3);
  if (b < 192) {
    const int row = tid >> 1;
    w0 = *(const u32x4*)(wmod + row * 3072 + seg * 16 + (tid & 1) * 8);
    w1 = *(const u32x4*)(wmod + (row + 256) * 3072 + seg * 16 + (tid & 1) * 8);
    c0 = c[row]; c1 = c[row + 256];
  }
  float v[8];
#pragma unroll
  for (int i = 0; i < 4; ++i) { v[2 * i] = lo2f(xv[i]); v[2 * i + 1] = hi2f(xv[i]); }
  float s = 0;
#pragma unroll
  for (int i = 0; i < 8; ++i) s += v[i];
  const float mean = wave_sum(s) * (1.f / DM);
  float q = 0;
#pragma unroll
  for (int i = 0; i < 8; ++i) { v[i] -= mean; q += v[i] * v[i]; }
  const float rstd = rsqrtf(wave_sum(q) * (1.f / DM) + 1e-5f);
  u32x4 xh;
#pragma unroll
  for (int i = 0; i < 4; ++i) xh[i] = pk2(v[2 * i] * rstd, v[2 * i + 1] * rstd);
  *(u32x4*)(ws + WS_XH + (size_t)tok * 1024 + lane * 16) = xh;
  {
    const u32 o = (lane >> 1) * 256 + wave * 16 + (lane & 1) * 8;
    *(LDSP u32x2*)((LDSP char*)smem + o) = u32x2{xv[0], xv[1]};
    *(LDSP u32x2*)((LDSP char*)smem + o + 128) = u32x2{xv[2], xv[3]};
  }
  if (b < 192) {
    const float f0 = bf2f(c0), f1 = bf2f(c1);
    const float s0 = rbf(f0 / (1.f + __expf(-f0))), s1 = rbf(f1 / (1.f + __expf(-f1)));
    float a[8];
#pragma unroll
    for (int i = 0; i < 4; ++i) {
      a[2 * i] = s0 * lo2f(w0[i]) + s1 * lo2f(w1[i]);
      a[2 * i + 1] = s0 * hi2f(w0[i]) + s1 * hi2f(w1[i]);
    }
#pragma unroll
    for (int i = 0; i < 8; ++i) { a[i] += dpp<0x4E>(a[i]); a[i] += dpp<0x124>(a[i]); a[i] += dpp<0x128>(a[i]); }
    if ((lane & 14) == 0) {
      const u32 o = 8192 + ((wave * 4 + (lane >> 4)) * 16 + (lane & 1) * 8) * 4;
      lds_wf4(o, f32x4{a[0], a[1], a[2], a[3]});
      lds_wf4(o + 16, f32x4{a[4], a[5], a[6], a[7]});
    }
  }
  BAR();
  {
    const u32x4 o = lds_r128(tid * 16);
    const int tt = b >> 2, r = (b & 3) * 8 + (tid & 7);
    *(u32x4*)(ws + WS_XF + (size_t)tt * 32768 + (tid >> 4) * 1024 + ((tid >> 3) & 1) * 512 + r * 16) = o;
  }
  if (b < 192 && tid < 16) {
    float a = 0;
#pragma unroll
    for (int i = 0; i < 32; ++i) a += lds_rf(8192 + (i * 16 + tid) * 4);
    a = rbf(a);
    const int col = seg * 16 + tid, part = col >> 9;
    if (part == 1 || part == 4) a = rbf(1.f + a);
    ((float*)(ws + WS_MOD))[col] = a;
  }
  const int gw = b * 8 + wave;
#pragma unroll
  for (int i = 0; i < 3; ++i) tile_frag(gw + i * 2048, lane, wq, wk, wv, wo, wg, wu, wd, ws);
  if (gw < 6432 - 6144) tile_frag(gw + 6144, lane, wq, wk, wv, wo, wg, wu, wd, ws);
}

constexpr u32 A_H = 0, A_QX = 65536, A_US = 98304;
constexpr u32 A_K = 0, A_V = 65536, A_ONES = 131072;
constexpr u32 A_KM = 131072 + 8192;
constexpr u32 K1_LDS = 131072 + 8192 + 256;
constexpr float QSCALE = 0.25f * LOG2E;
constexpr float BOUND2 = 64.f * 64.f;

#define EXP2(x) __builtin_amdgcn_exp2f(x)

#define HALF(Sc, Sn, Kn, Qn, Oc, V0, V1, CIN)                                                          \
  do {                                                                                                 \
    u32x4 p0_, p1_;                                                                                    \
    Sc[0] = EXP2(Sc[0]); Sc[1] = EXP2(Sc[1]); Sc[2] = EXP2(Sc[2]); Sc[3] = EXP2(Sc[3]);                \
    p0_[0] = pk2(Sc[0], Sc[1]); p0_[1] = pk2(Sc[2], Sc[3]); PIN(p0_[0]); PIN(p0_[1]); SBAR();          \
    Sn = MFMA32(Kn, Qn, CIN); SBAR();                                                                  \
    Sc[4] = EXP2(Sc[4]); Sc[5] = EXP2(Sc[5]); Sc[6] = EXP2(Sc[6]); Sc[7] = EXP2(Sc[7]);                \
    p0_[2] = pk2(Sc[4], Sc[5]); p0_[3] = pk2(Sc[6], Sc[7]); PIN(p0_); SBAR();                          \
    Oc = MFMA32(V0, p0_, Oc); SBAR();                                                                  \
    Sc[8] = EXP2(Sc[8]); Sc[9] = EXP2(Sc[9]); Sc[10] = EXP2(Sc[10]); Sc[11] = EXP2(Sc[11]);            \
    p1_[0] = pk2(Sc[8], Sc[9]); p1_[1] = pk2(Sc[10], Sc[11]); PIN(p1_[0]); PIN(p1_[1]); SBAR();        \
    Sc[12] = EXP2(Sc[12]); Sc[13] = EXP2(Sc[13]); Sc[14] = EXP2(Sc[14]); Sc[15] = EXP2(Sc[15]);        \
    p1_[2] = pk2(Sc[12], Sc[13]); p1_[3] = pk2(Sc[14], Sc[15]); PIN(p1_); SBAR();                      \
    Oc = MFMA32(V1, p1_, Oc); SBAR();                                                                  \
  } while (0)

__global__ __launch_bounds__(512, 2) void k1_attn(char* __restrict__ ws) {
  const int tid = threadIdx.x, lane = tid & 63, b = blockIdx.x;
  const int wave = __builtin_amdgcn_readfirstlane(tid >> 6);
  const int r = lane & 31, hh = lane >> 5;
  const int idx = b >> 3, tg = (b & 7) * 4 + (idx >> 3), hg = idx & 7;
  const int th = wave & 1, hp = (wave >> 1) & 1, kh = wave >> 2;
  const float* mod = (const float*)(ws + WS_MOD);

  u32x4 wkv[16];
  if (wave == 7) {
#pragma unroll
    for (int i = 0; i < 16; ++i) wkv[i] = *(const u32x4*)(ws + WS_WKVT + (size_t)((hg & 1) * 16 + i) * 1024 + lane * 16);
  }
  u32x4 xr[8];
#pragma unroll
  for (int i = 0; i < 8; ++i) xr[i] = *(const u32x4*)(ws + WS_XH + (size_t)(64 * tg + 8 * wave + i) * 1024 + lane * 16);
  const f32x4 sa0 = *(const f32x4*)(mod + 8 * lane), sa1 = *(const f32x4*)(mod + 8 * lane + 4);
  const f32x4 ca0 = *(const f32x4*)(mod + 512 + 8 * lane), ca1 = *(const f32x4*)(mod + 512 + 8 * lane + 4);
  u32x4 wq[16];
#pragma unroll
  for (int i = 0; i < 16; ++i) wq[i] = *(const u32x4*)(ws + WS_WQT + (size_t)((2 * hg + hp) * 32 + 16 * kh + i) * 1024 + lane * 16);
  u32x4 w1[4], mb[4];
  if (b < 112) {
#pragma unroll
    for (int i = 0; i < 4; ++i) {
      w1[i] = *(const u32x4*)(ws + WS_W1T + (size_t)(b * 32 + 4 * wave + i) * 1024 + lane * 16);
      u32x4 t = {0, 0, 0, 0};
      if (r < 2) {
        const float* m = mod + (r == 0 ? 2048 : 1536) + 16 * (4 * wave + i) + 4 * hh;
        const f32x4 a = *(const f32x4*)m, c = *(const f32x4*)(m + 8);
        t = u32x4{pk2(a[0], a[1]), pk2(a[2], a[3]), pk2(c[0], c[1]), pk2(c[2], c[3])};
      }
      mb[i] = t;
    }
  }
  lds_w128(A_ONES + tid * 16, u32x4{0x3f803f80u, 0x3f803f80u, 0x3f803f80u, 0x3f803f80u});
  {
    u32 cw[8]; float sa[8];
#pragma unroll
    for (int i = 0; i < 4; ++i) {
      sa[i] = sa0[i]; sa[4 + i] = sa1[i];
    }
    cw[0] = __float_as_uint(ca0[0]) >> 16; cw[1] = __float_as_uint(ca0[1]); cw[2] = __float_as_uint(ca0[2]) >> 16; cw[3] = __float_as_uint(ca0[3]);
    cw[4] = __float_as_uint(ca1[0]) >> 16; cw[5] = __float_as_uint(ca1[1]); cw[6] = __float_as_uint(ca1[2]) >> 16; cw[7] = __float_as_uint(ca1[3]);
#pragma unroll
    for (int i = 0; i < 8; ++i) {
      const int rho = 8 * wave + i, rr = rho & 31;
      float t0, t1, t2, t3, t4, t5, t6, t7;
      asm("v_dot2_f32_bf16 %0, %8, %12, %20\n\tv_dot2_f32_bf16 %1, %8, %13, %21\n\t"
          "v_dot2_f32_bf16 %2, %9, %14, %22\n\tv_dot2_f32_bf16 %3, %9, %15, %23\n\t"
          "v_dot2_f32_bf16 %4, %10, %16, %24\n\tv_dot2_f32_bf16 %5, %10, %17, %25\n\t"
          "v_dot2_f32_bf16 %6, %11, %18, %26\n\tv_dot2_f32_bf16 %7, %11, %19, %27\n\ts_nop 2"
          : "=&v"(t0), "=&v"(t1), "=&v"(t2), "=&v"(t3), "=&v"(t4), "=&v"(t5), "=&v"(t6), "=&v"(t7)
          : "v"(xr[i][0]), "v"(xr[i][1]), "v"(xr[i][2]), "v"(xr[i][3]),
            "v"(cw[0]), "v"(cw[1]), "v"(cw[2]), "v"(cw[3]), "v"(cw[4]), "v"(cw[5]), "v"(cw[6]), "v"(cw[7]),
            "v"(sa[0]), "v"(sa[1]), "v"(sa[2]), "v"(sa[3]), "v"(sa[4]), "v"(sa[5]), "v"(sa[6]), "v"(sa[7]));
      const u32x4 hv = {pk2(t0, t1), pk2(t2, t3), pk2(t4, t5), pk2(t6, t7)};
      lds_w128(A_H + (rho >> 5) * 32768 + (lane >> 1) * 1024 + (lane & 1) * 512 + ((rr ^ (lane & 15)) * 16), hv);
    }
  }
  BAR();
  if (wave == 7) {
    const int g = lane >> 4, i = lane & 15, q = hg >> 1, m = hg & 1;
    const int rr = (q & 1) * 16 + i;
    f32x4 d0 = {0, 0, 0, 0}, d1 = {0, 0, 0, 0};
#pragma unroll
    for (int t = 0; t < 16; ++t) {
      const int cch = 4 * t + g;
      const u32x4 hf = lds_r128(A_H + (q >> 1) * 32768 + (cch >> 1) * 1024 + (cch & 1) * 512 + ((rr ^ (cch & 15)) * 16));
      if (m == 0) { if (t & 1) d1 = MFMA16(wkv[t], hf, d1); else d0 = MFMA16(wkv[t], hf, d0); }
      else        { if (t & 1) d1 = MFMA16(hf, wkv[t], d1); else d0 = MFMA16(hf, wkv[t], d0); }
    }
    d0 += d1;
    const u32x2 pkd = {pk2(d0[0], d0[1]), pk2(d0[2], d0[3])};
    const int T = 2 * tg + (q >> 1);
    if (m == 0) {
      st8_sc1(ws + WS_KF + T * 1024 + (g & 1) * 512 + rr * 16 + (g >> 1) * 8, pkd);
      const float k0 = lo2f(pkd[0]), k1 = hi2f(pkd[0]), k2 = lo2f(pkd[1]), k3 = hi2f(pkd[1]);
      float n2 = k0 * k0 + k1 * k1 + k2 * k2 + k3 * k3;
      n2 = row_max(swap32_sum(swap16_sum(n2)));
      if (lane < NREP) __hip_atomic_fetch_max((GLBP u32*)(ws + WS_CNT + lane * 128 + 4), __float_as_uint(n2), __ATOMIC_RELAXED, __HIP_MEMORY_SCOPE_AGENT);
    } else {
      st8_sc1(ws + WS_VF + T * 1024 + (q & 1) * 512 + (g & 1) * 256 + i * 16 + (g >> 1) * 8, pkd);
    }
    WAITV(0);
    if (lane < NREP) __hip_atomic_fetch_add((GLBP u32*)(ws + WS_CNT + lane * 128), 1u, __ATOMIC_RELAXED, __HIP_MEMORY_SCOPE_AGENT);
  }
  f32x16 qa = {};
#pragma unroll
  for (int i = 0; i < 16; ++i) {
    const int ks = 16 * kh + i, cch = 2 * ks + hh;
    const u32x4 hf = lds_r128(A_H + th * 32768 + ks * 1024 + hh * 512 + ((r ^ (cch & 15)) * 16));
    qa = MFMA32(wq[i], hf, qa);
  }
#pragma unroll
  for (int i = 0; i < 4; ++i) lds_wf4(A_QX + wave * 4096 + i * 1024 + lane * 16, f32x4{qa[4 * i], qa[4 * i + 1], qa[4 * i + 2], qa[4 * i + 3]});
  if (b < 112) {
    f32x16 us = {};
#pragma unroll
    for (int i = 0; i < 4; ++i) us = MFMA32(w1[i], mb[i], us);
    if (r < 2) {
#pragma unroll
      for (int i = 0; i < 4; ++i)
        lds_wf4(A_US + wave * 256 + r * 128 + (8 * i + 4 * hh) * 4, f32x4{us[4 * i], us[4 * i + 1], us[4 * i + 2], us[4 * i + 3]});
    }
  }
  BAR();
  u32x4 Qa, Qb; float qn2;
  {
    float t[16];
#pragma unroll
    for (int i = 0; i < 4; ++i) {
      const f32x4 o = lds_rf4(A_QX + (wave ^ 4) * 4096 + i * 1024 + lane * 16);
#pragma unroll
      for (int j = 0; j < 4; ++j) t[4 * i + j] = (qa[4 * i + j] + o[j]) * QSCALE;
    }
#pragma unroll
    for (int j = 0; j < 4; ++j) { Qa[j] = pk2(t[2 * j], t[2 * j + 1]); Qb[j] = pk2(t[8 + 2 * j], t[9 + 2 * j]); }
    float na = 0, nb = 0;
#pragma unroll
    for (int j = 0; j < 4; ++j) {
      na += lo2f(Qa[j]) * lo2f(Qa[j]) + hi2f(Qa[j]) * hi2f(Qa[j]);
      nb += lo2f(Qb[j]) * lo2f(Qb[j]) + hi2f(Qb[j]) * hi2f(Qb[j]);
    }
    qn2 = fmaxf(swap32_sum(na), swap32_sum(nb));
  }
  if (b < 112 && wave == 1) {
    float a = 0;
#pragma unroll
    for (int w = 0; w < 8; ++w) a += lds_rf(A_US + w * 256 + lane * 4);
    if (lane >= 32 && lane < 48) a *= -LOG2E;
    ((float*)(ws + WS_US))[b * 64 + lane] = a;
  }
  if (wave == 0) {
    if (lane == 0) {
      const char* p = ws + WS_CNT + (b & (NREP - 1)) * 128;
      unsigned long long cv = 0;
      for (int spin = 0; spin < (1 << 20); ++spin) {
        cv = __hip_atomic_load((GLBP unsigned long long*)p, __ATOMIC_RELAXED, __HIP_MEMORY_SCOPE_AGENT);
        if ((u32)cv >= 256u) break;
        __builtin_amdgcn_s_sleep(1);
      }
      lds_wf(A_KM, __uint_as_float((u32)(cv >> 32)));
    }
  }
  asm volatile("s_waitcnt vmcnt(0) lgkmcnt(0)\n\ts_barrier" ::: "memory");
  const u32 lb = lds_base();
  {
    const u32 vo = lane * 16;
#pragma unroll
    for (int c = 0; c < 4; ++c) {
      const int t0 = 8 * c + wave, t1 = 32 + t0;
      glds1_sc1(ws + WS_KF + t0 * 1024, vo, lb + A_K + t0 * 1024);
      glds1_sc1(ws + WS_VF + t0 * 1024, vo, lb + A_V + t0 * 1024);
      glds1_sc1(ws + WS_KF + t1 * 1024, vo, lb + A_K + t1 * 1024);
      glds1_sc1(ws + WS_VF + t1 * 1024, vo, lb + A_V + t1 * 1024);
    }
  }
  const float km2 = lds_rf(A_KM);
  const bool safe = !__any(!(qn2 * km2 <= BOUND2));
  WAITV_BAR(0);

  f32x16 Oa = {}, Ob = {};
  const u32 kadr = A_K + kh * 32768 + lane * 16;
  u32 vadr = (r < 16) ? (A_V + kh * 32768 + hh * 256 + r * 16) : (A_ONES + ((r >= 20 && r < 28) ? 64 : 0));
  const u32 vstep = (r < 16) ? 1024u : 0u;
  if (safe) {
    f32x16 Sa, Sb;
    const f32x16 zero = {};
    u32x4 Kc = lds_r128(kadr);
    Sa = MFMA32(Kc, Qa, zero);
    u32x4 V0 = lds_r128(vadr), V1 = lds_r128(vadr + 512);
#pragma unroll 1
    for (int t = 0; t < 32; t += 2) {
      const u32 ka = kadr + t * 1024;
      u32x4 Kn = lds_r128(ka + 1024);
      u32x4 W0 = lds_r128(vadr + 1024), W1 = lds_r128(vadr + 1536);
      SBAR();
      HALF(Sa, Sb, Kc, Qb, Oa, V0, V1, zero);
      HALF(Sb, Sa, Kn, Qa, Ob, V0, V1, zero);
      vadr += 2 * vstep;
      Kc = lds_r128(ka + 2048);
      V0 = lds_r128(vadr); V1 = lds_r128(vadr + 512);
      SBAR();
      HALF(Sa, Sb, Kn, Qb, Oa, W0, W1, zero);
      HALF(Sb, Sa, Kc, Qa, Ob, W0, W1, zero);
    }
  } else {
#pragma unroll 1
    for (int x2 = 0; x2 < 2; ++x2) {
      const u32x4 Q = x2 ? Qb : Qa;
      float m = -3.0e38f;
      const f32x16 zero = {};
#pragma unroll 1
      for (int t = 0; t < 64; ++t) {
        const f32x16 s = MFMA32(lds_r128(A_K + t * 1024 + lane * 16), Q, zero);
#pragma unroll
        for (int j = 0; j < 16; ++j) m = fmaxf(m, s[j]);
      }
      m = swap32_max(m);
      f32x16 cm;
#pragma unroll
      for (int j = 0; j < 16; ++j) cm[j] = -m;
      f32x16 O = {};
      u32 va = vadr;
#pragma unroll 1
      for (int t = 0; t < 32; ++t) {
        f32x16 s = MFMA32(lds_r128(kadr + t * 1024), Q, cm);
        u32x4 p0, p1;
#pragma unroll
        for (int j = 0; j < 4; ++j) { p0[j] = pk2(EXP2(s[2 * j]), EXP2(s[2 * j + 1])); p1[j] = pk2(EXP2(s[8 + 2 * j]), EXP2(s[9 + 2 * j])); }
        O = MFMA32(lds_r128(va), p0, O);
        O = MFMA32(lds_r128(va + 512), p1, O);
        va += vstep;
      }
      if (x2) Ob = O; else Oa = O;
    }
  }
  BAR();
  {
    const u32 o = wave * 3072 + lane * 16;
    if (kh == 0) {
      lds_wf4(o, f32x4{Ob[0], Ob[1], Ob[2], Ob[3]}); lds_wf4(o + 1024, f32x4{Ob[4], Ob[5], Ob[6], Ob[7]}); lds_wf(2048 + wave * 3072 + lane * 4, Ob[8]);
    } else {
      lds_wf4(o, f32x4{Oa[0], Oa[1], Oa[2], Oa[3]}); lds_wf4(o + 1024, f32x4{Oa[4], Oa[5], Oa[6], Oa[7]}); lds_wf(2048 + wave * 3072 + lane * 4, Oa[8]);
    }
  }
  BAR();
  {
    const u32 o = (wave ^ 4) * 3072 + lane * 16;
    const f32x4 a = lds_rf4(o), c = lds_rf4(o + 1024);
    const float l2 = lds_rf(2048 + (wave ^ 4) * 3072 + lane * 4);
    float t[8], l;
    if (kh == 0) {
#pragma unroll
      for (int j = 0; j < 4; ++j) { t[j] = Oa[j] + a[j]; t[4 + j] = Oa[4 + j] + c[j]; }
      l = Oa[8] + l2;
    } else {
#pragma unroll
      for (int j = 0; j < 4; ++j) { t[j] = Ob[j] + a[j]; t[4 + j] = Ob[4 + j] + c[j]; }
      l = Ob[8] + l2;
    }
    const float il = 1.f / l;
    const u32x4 ov = {pk2(t[0] * il, t[1] * il), pk2(t[2] * il, t[3] * il), pk2(t[4] * il, t[5] * il), pk2(t[6] * il, t[7] * il)};
    const int head = 4 * hg + 2 * hp + kh;
    *(u32x4*)(ws + WS_OF + (size_t)((2 * tg + th) * 32 + head) * 1024 + lane * 16) = ov;
  }
}

constexpr u32 K2_LDS = 131072 + 16384 + 4096;
__global__ __launch_bounds__(512, 2) void k2_wo(char* __restrict__ ws) {
  const int tid = threadIdx.x, lane = tid & 63, b = blockIdx.x;
  const int wave = __builtin_amdgcn_readfirstlane(tid >> 6);
  const int r = lane & 31, hh = lane >> 5;
  const int idx = b >> 3, tg = (b & 7) * 4 + (idx >> 3), ng = idx & 7;
  const int th = wave & 1, nh = (wave >> 1) & 1, kh = wave >> 2;
  const float* mod = (const float*)(ws + WS_MOD);
  const u32 lb = lds_base(), vo = lane * 16;
  const int ksf = 2 * (2 * ng + nh) + kh;
  const u32x4 xf = *(const u32x4*)(ws + WS_XF + (size_t)((2 * tg + th) * 32 + ksf) * 1024 + lane * 16);
  const int n0 = 16 * ksf + 4 * hh;
  const f32x4 ga0 = *(const f32x4*)(mod + 1024 + n0), ga1 = *(const f32x4*)(mod + 1024 + n0 + 8);
  const f32x4 cb0 = *(const f32x4*)(mod + 2048 + n0), cb1 = *(const f32x4*)(mod + 2048 + n0 + 8);
#pragma unroll
  for (int c = 0; c < 4; ++c) {
    const int mat = wave >> 2, row = (wave >> 1) & 1, ks = 16 * (wave & 1) + 4 * c;
    const char* g = mat ? ws + WS_OF + (size_t)((2 * tg + row) * 32 + ks) * 1024 : ws + WS_WOT + (size_t)((2 * ng + row) * 32 + ks) * 1024;
    glds4(g, vo, lb + mat * 65536 + row * 32768 + ks * 1024);
  }
  f32x16 acc = {};
  const u32 aa = nh * 32768 + kh * 16384 + lane * 16, ba = 65536 + th * 32768 + kh * 16384 + lane * 16;
#define K2_CHUNK(c, N)                                                                           \
  asm volatile("s_waitcnt vmcnt(" #N ")\n\ts_barrier" ::: "memory");                             \
  _Pragma("unroll") for (int i = 0; i < 4; ++i) acc = MFMA32(lds_r128(aa + (4 * c + i) * 1024), lds_r128(ba + (4 * c + i) * 1024), acc);
  K2_CHUNK(0, 12) K2_CHUNK(1, 8) K2_CHUNK(2, 4) K2_CHUNK(3, 0)
#undef K2_CHUNK
  {
    const u32 o = 131072 + wave * 2048 + lane * 16;
    if (kh == 0) { lds_wf4(o, f32x4{acc[8], acc[9], acc[10], acc[11]}); lds_wf4(o + 1024, f32x4{acc[12], acc[13], acc[14], acc[15]}); }
    else         { lds_wf4(o, f32x4{acc[0], acc[1], acc[2], acc[3]});   lds_wf4(o + 1024, f32x4{acc[4], acc[5], acc[6], acc[7]}); }
  }
  BAR();
  float t[8];
  {
    const u32 o = 131072 + (wave ^ 4) * 2048 + lane * 16;
    const f32x4 a = lds_rf4(o), c = lds_rf4(o + 1024);
#pragma unroll
    for (int j = 0; j < 4; ++j) {
      t[j] = (kh ? acc[8 + j] : acc[j]) + a[j];
      t[4 + j] = (kh ? acc[12 + j] : acc[4 + j]) + c[j];
    }
  }
  u32x4 x2, y2; float xv[8];
#pragma unroll
  for (int j = 0; j < 2; ++j) {
    x2[j] = pk2(lo2f(xf[j]) + ga0[2 * j] * t[2 * j], hi2f(xf[j]) + ga0[2 * j + 1] * t[2 * j + 1]);
    x2[2 + j] = pk2(lo2f(xf[2 + j]) + ga1[2 * j] * t[4 + 2 * j], hi2f(xf[2 + j]) + ga1[2 * j + 1] * t[5 + 2 * j]);
  }
#pragma unroll
  for (int j = 0; j < 4; ++j) { xv[2 * j] = lo2f(x2[j]); xv[2 * j + 1] = hi2f(x2[j]); }
#pragma unroll
  for (int j = 0; j < 2; ++j) {
    y2[j] = pk2(xv[2 * j] * cb0[2 * j], xv[2 * j + 1] * cb0[2 * j + 1]);
    y2[2 + j] = pk2(xv[4 + 2 * j] * cb1[2 * j], xv[5 + 2 * j] * cb1[2 * j + 1]);
  }
  const size_t fo = (size_t)((2 * tg + th) * 32 + ksf) * 1024 + lane * 16;
  *(u32x4*)(ws + WS_X2F + fo) = x2;
  *(u32x4*)(ws + WS_Y2F + fo) = y2;
  float s = 0;
#pragma unroll
  for (int j = 0; j < 8; ++j) s += xv[j];
  const float m16 = swap32_sum(s) * (1.f / 16.f);
  float q = 0;
#pragma unroll
  for (int j = 0; j < 8; ++j) { const float d = xv[j] - m16; q += d * d; }
  q = swap32_sum(q);
  const u32 so = 131072 + 16384;
  if (hh == 0) *(LDSP f32x2*)((LDSP char*)smem + so + ((th * 32 + r) * 4 + (wave >> 1)) * 8) = f32x2{m16, q};
  BAR();
  if (tid < 64) {
    const f32x4 a = lds_rf4(so + tid * 32), c = lds_rf4(so + tid * 32 + 16);
    const float m = 0.25f * (a[0] + a[2] + c[0] + c[2]);
    const float d0 = a[0] - m, d1 = a[2] - m, d2 = c[0] - m, d3 = c[2] - m;
    const float M2 = a[1] + a[3] + c[1] + c[3] + 16.f * (d0 * d0 + d1 * d1 + d2 * d2 + d3 * d3);
    *(f32x2*)(ws + WS_ST + (size_t)(64 * tg + tid) * 64 + ng * 8) = f32x2{m, M2};
  }
}

constexpr u32 K3_LDS = 131072 + 4096;
__global__ __launch_bounds__(512, 2) void k3_ffn1(char* __restrict__ ws) {
  const int tid = threadIdx.x, lane = tid & 63, b = blockIdx.x;
  const int wave = __builtin_amdgcn_readfirstlane(tid >> 6);
  const int r = lane & 31, hh = lane >> 5;
  const int idx = b >> 3, G = (b & 7) * 2 + (idx >> 4), cs = idx & 15;
  const u32 lb = lds_base(), vo = lane * 16;
#pragma unroll
  for (int tt = 0; tt < 4; ++tt)
    glds4(ws + WS_Y2F + (size_t)((4 * G + tt) * 32 + 4 * wave) * 1024, vo, lb + tt * 32768 + wave * 4096);
  const int a = 7 * cs + (wave < 7 ? wave : 6);
  u32x4 w[32];
  if (wave < 7) {
#pragma unroll
    for (int i = 0; i < 32; ++i) w[i] = *(const u32x4*)(ws + WS_W1T + (size_t)(a * 32 + i) * 1024 + lane * 16);
  } else {
#pragma unroll
    for (int h2 = 0; h2 < 2; ++h2) {
      const int t = h2 * 64 + lane;
      const float* p = (const float*)(ws + WS_ST + (size_t)(128 * G + t) * 64);
      f32x4 v[4];
#pragma unroll
      for (int i = 0; i < 4; ++i) v[i] = *(const f32x4*)(p + 4 * i);
      float m = 0, M2 = 0;
#pragma unroll
      for (int i = 0; i < 4; ++i) { m += v[i][0] + v[i][2]; M2 += v[i][1] + v[i][3]; }
      m *= 0.125f;
#pragma unroll
      for (int i = 0; i < 4; ++i) { const float d0 = v[i][0] - m, d1 = v[i][2] - m; M2 += 64.f * (d0 * d0 + d1 * d1); }
      const float rs = rsqrtf(M2 * (1.f / DM) + 1e-5f);
      *(LDSP f32x2*)((LDSP char*)smem + 131072 + t * 8) = f32x2{rs, m * rs};
    }
  }
  const float* us = (const float*)(ws + WS_US) + a * 64;
  f32x4 ug[2], uu[2], sg[2], su[2];
#pragma unroll
  for (int i = 0; i < 2; ++i) {
    ug[i] = *(const f32x4*)(us + 8 * i + 4 * hh);      uu[i] = *(const f32x4*)(us + 16 + 8 * i + 4 * hh);
    sg[i] = *(const f32x4*)(us + 32 + 8 * i + 4 * hh); su[i] = *(const f32x4*)(us + 48 + 8 * i + 4 * hh);
  }
  WAITV_BAR(0);
  if (wave < 7) {
#pragma unroll
    for (int tt = 0; tt < 4; ++tt) {
      f32x16 acc = {};
#pragma unroll
      for (int i = 0; i < 32; ++i) acc = MFMA32(w[i], lds_r128(tt * 32768 + i * 1024 + lane * 16), acc);
      const f32x2 st = *(const LDSP f32x2*)((LDSP char*)smem + 131072 + (tt * 32 + r) * 8);
      const float rs = st[0], mr = st[1];
      const float rg = -LOG2E * rs, mg = LOG2E * mr;
      float hv[8];
#pragma unroll
      for (int j = 0; j < 8; ++j) {
        const float g = rg * acc[j] + (mg * ug[j >> 2][j & 3] + sg[j >> 2][j & 3]);
        const float u = rs * acc[8 + j] + (su[j >> 2][j & 3] - mr * uu[j >> 2][j & 3]);
        hv[j] = u * __builtin_amdgcn_rcpf(1.f + EXP2(g));
      }
      const u32x4 o = {pk2(hv[0], hv[1]), pk2(hv[2], hv[3]), pk2(hv[4], hv[5]), pk2(hv[6], hv[7])};
      *(u32x4*)(ws + WS_HF + (size_t)((4 * G + tt) * 112 + a) * 1024 + lane * 16) = o;
    }
  }
}

constexpr u32 K4_LDS = 131072 + 16384;
__global__ __launch_bounds__(512, 2) void k4_ffn2(const char* __restrict__ ws, u16* __restrict__ out) {
  const int tid = threadIdx.x, lane = tid & 63, b = blockIdx.x;
  const int wave = __builtin_amdgcn_readfirstlane(tid >> 6);
  const int r = lane & 31, hh = lane >> 5;
  const int idx = b >> 3, tg = (b & 7) * 4 + (idx >> 3), ng = idx & 7;
  const int th = wave & 1, nh = (wave >> 1) & 1, kh = wave >> 2;
  const float* mod = (const float*)(ws + WS_MOD);
  const u32 lb = lds_base(), vo = lane * 16;
  const int mat = wave >> 2, row = (wave >> 1) & 1, half = wave & 1;
  const char* gsrc = (mat ? ws + WS_HF + (size_t)((2 * tg + row) * 112) * 1024 : ws + WS_WDT + (size_t)((2 * ng + row) * 112) * 1024) + half * 56 * 1024;
  const u32 ldst = lb + wave * 4096;
  const int ksf = 2 * (2 * ng + nh) + kh;
  const u32x4 xf = *(const u32x4*)(ws + WS_X2F + (size_t)((2 * tg + th) * 32 + ksf) * 1024 + lane * 16);
  const int n0 = 16 * ksf + 4 * hh;
  const f32x4 gb0 = *(const f32x4*)(mod + 2560 + n0), gb1 = *(const f32x4*)(mod + 2560 + n0 + 8);
  glds4(gsrc, vo, ldst);
  glds4(gsrc + 4096, vo, ldst + 32768);
  glds4(gsrc + 8192, vo, ldst + 65536);
  f32x16 acc = {};
  const u32 aa = (nh * 2 + kh) * 4096 + lane * 16, ba = 16384 + (th * 2 + kh) * 4096 + lane * 16;
#pragma unroll 1
  for (int c = 0; c < 14; ++c) {
    if (c < 12) asm volatile("s_waitcnt vmcnt(8) lgkmcnt(0)\n\ts_barrier" ::: "memory");
    else if (c == 12) asm volatile("s_waitcnt vmcnt(4) lgkmcnt(0)\n\ts_barrier" ::: "memory");
    else asm volatile("s_waitcnt vmcnt(0) lgkmcnt(0)\n\ts_barrier" ::: "memory");
    if (c + 3 < 14) glds4(gsrc + (c + 3) * 4096, vo, ldst + ((c + 3) & 3) * 32768);
    const u32 so = (c & 3) * 32768;
#pragma unroll
    for (int i = 0; i < 4; ++i) acc = MFMA32(lds_r128(so + aa + i * 1024), lds_r128(so + ba + i * 1024), acc);
  }
  {
    const u32 o = 131072 + wave * 2048 + lane * 16;
    if (kh == 0) { lds_wf4(o, f32x4{acc[8], acc[9], acc[10], acc[11]}); lds_wf4(o + 1024, f32x4{acc[12], acc[13], acc[14], acc[15]}); }
    else         { lds_wf4(o, f32x4{acc[0], acc[1], acc[2], acc[3]});   lds_wf4(o + 1024, f32x4{acc[4], acc[5], acc[6], acc[7]}); }
  }
  BAR();
  float t[8];
  {
    const u32 o = 131072 + (wave ^ 4) * 2048 + lane * 16;
    const f32x4 a = lds_rf4(o), c = lds_rf4(o + 1024);
#pragma unroll
    for (int j = 0; j < 4; ++j) {
      t[j] = (kh ? acc[8 + j] : acc[j]) + a[j];
      t[4 + j] = (kh ? acc[12 + j] : acc[4 + j]) + c[j];
    }
  }
  u32 p0 = pk2(lo2f(xf[0]) + gb0[0] * t[0], hi2f(xf[0]) + gb0[1] * t[1]);
  u32 p1 = pk2(lo2f(xf[1]) + gb0[2] * t[2], hi2f(xf[1]) + gb0[3] * t[3]);
  u32 p2 = pk2(lo2f(xf[2]) + gb1[0] * t[4], hi2f(xf[2]) + gb1[1] * t[5]);
  u32 p3 = pk2(lo2f(xf[3]) + gb1[2] * t[6], hi2f(xf[3]) + gb1[3] * t[7]);
  auto s0 = __builtin_amdgcn_permlane32_swap(p0, p2, false, false);
  auto s1 = __builtin_amdgcn_permlane32_swap(p1, p3, false, false);
  const u32x4 ov = {s0[0], s1[0], s0[1], s1[1]};
  *(u32x4*)(out + (size_t)(64 * tg + 32 * th + r) * DM + 16 * ksf + 8 * hh) = ov;
}

extern "C" void kernel_launch(void* const* d_in, const int* in_sizes, int n_in, void* d_out, int out_size, void* d_ws, size_t ws_size,
                              hipStream_t stream) {
  const u16 *x = (const u16*)d_in[0], *wq = (const u16*)d_in[1], *wk = (const u16*)d_in[2], *wv = (const u16*)d_in[3], *wo = (const u16*)d_in[4],
            *wg = (const u16*)d_in[6], *wu = (const u16*)d_in[7], *wd = (const u16*)d_in[8], *c = (const u16*)d_in[9], *wm = (const u16*)d_in[10];
  char* ws = (char*)d_ws;
  (void)hipFuncSetAttribute((const void*)k1_attn, hipFuncAttributeMaxDynamicSharedMemorySize, K1_LDS);
  (void)hipFuncSetAttribute((const void*)k2_wo, hipFuncAttributeMaxDynamicSharedMemorySize, K2_LDS);
  (void)hipFuncSetAttribute((const void*)k3_ffn1, hipFuncAttributeMaxDynamicSharedMemorySize, K3_LDS);
  (void)hipFuncSetAttribute((const void*)k4_ffn2, hipFuncAttributeMaxDynamicSharedMemorySize, K4_LDS);
  k0_prep<<<256, 512, 8192 + 2048, stream>>>(x, c, wm, wq, wk, wv, wo, wg, wu, wd, ws);
  k1_attn<<<256, 512, K1_LDS, stream>>>(ws);
  k2_wo<<<256, 512, K2_LDS, stream>>>(ws);
  k3_ffn1<<<256, 512, K3_LDS, stream>>>(ws);
  k4_ffn2<<<256, 512, K4_LDS, stream>>>((const char*)ws, (u16*)d_out);
}
```

```cpp
#include <hip/hip_runtime.h>
#include <hip/hip_bf16.h>
#include <stdint.h>

typedef unsigned short u16;
typedef unsigned int u32;
typedef unsigned long long u64;
typedef __bf16 bf16x8 __attribute__((ext_vector_type(8)));
typedef __bf16 bf16x2 __attribute__((ext_vector_type(2)));
typedef short s16x4 __attribute__((ext_vector_type(4)));
typedef float f32x16 __attribute__((ext_vector_type(16)));
typedef float f32x4 __attribute__((ext_vector_type(4)));
typedef float f32x2 __attribute__((ext_vector_type(2)));
typedef u32 u32x4 __attribute__((ext_vector_type(4)));
typedef u32 u32x2 __attribute__((ext_vector_type(2)));
#define LDSP __attribute__((address_space(3)))
#define GLBP __attribute__((address_space(1)))

constexpr int SEQ = 2048, DM = 512, FF = 1792;
constexpr float LOG2E = 1.4426950408889634f;

constexpr u32 WS_EPOCH = 0;
constexpr u32 WS_KVFL  = 1024;
constexpr u32 WS_PREP  = 4096;
constexpr u32 WS_OFL   = 8192;
constexpr u32 WS_YFL   = 12288;
constexpr u32 WS_HFL   = 16384;
constexpr u32 WS_MODG  = 20480;
constexpr u32 WS_ST    = 65536;
constexpr u32 WS_KF    = WS_ST + 128 * 16 * 128;
constexpr u32 WS_VF    = WS_KF + 65536;
constexpr u32 WS_WOT   = WS_VF + 65536;
constexpr u32 WS_W1T   = WS_WOT + 524288;
constexpr u32 WS_WDT   = WS_W1T + 3584 * 1024;
constexpr u32 WS_OF    = WS_WDT + 1792 * 1024;
constexpr u32 WS_Y2F   = WS_OF + 2097152;
constexpr u32 WS_HF    = WS_Y2F + 2097152;
constexpr u32 WS_END   = WS_HF + 64 * 112 * 1024;

__device__ __forceinline__ float bf2f(u32 v) { return __uint_as_float(v << 16); }
__device__ __forceinline__ float lo2f(u32 w) { return __uint_as_float(w << 16); }
__device__ __forceinline__ float hi2f(u32 w) { return __uint_as_float(w & 0xffff0000u); }
__device__ __forceinline__ u32 pk2(float a, float b) {
  f32x2 v = {a, b};
  return __builtin_bit_cast(u32, __builtin_convertvector(v, bf16x2));
}
__device__ __forceinline__ float rbf(float a) { return lo2f(pk2(a, a)); }
__device__ __forceinline__ bf16x8 asbf(u32x4 v) { return __builtin_bit_cast(bf16x8, v); }
#define MFMA32(a, b, c) __builtin_amdgcn_mfma_f32_32x32x16_bf16(asbf(a), asbf(b), c, 0, 0, 0)
#define MFMA16(a, b, c) __builtin_amdgcn_mfma_f32_16x16x32_bf16(asbf(a), asbf(b), c, 0, 0, 0)
#define SBAR() __builtin_amdgcn_sched_barrier(0)
#define PIN(x) asm volatile("" : "+v"(x))
#define BAR() asm volatile("s_waitcnt lgkmcnt(0)\n\ts_barrier" ::: "memory")
#define WAITV(N) asm volatile("s_waitcnt vmcnt(" #N ")" ::: "memory")
#define WAITV_BAR(N) asm volatile("s_waitcnt vmcnt(" #N ") lgkmcnt(0)\n\ts_barrier" ::: "memory")
#define RLX_AGENT __ATOMIC_RELAXED, __HIP_MEMORY_SCOPE_AGENT
constexpr int SC1 = 16;
constexpr int SPIN_CAP = 1 << 16;

template <int CTRL>
__device__ __forceinline__ float dpp(float x) {
  return __int_as_float(__builtin_amdgcn_update_dpp(0, __float_as_int(x), CTRL, 0xf, 0xf, true));
}
__device__ __forceinline__ float row_sum(float x) {
  x += dpp<0x128>(x); x += dpp<0x124>(x); x += dpp<0x122>(x); x += dpp<0x121>(x); return x;
}
__device__ __forceinline__ float row_max(float x) {
  x = fmaxf(x, dpp<0x128>(x)); x = fmaxf(x, dpp<0x124>(x)); x = fmaxf(x, dpp<0x122>(x)); x = fmaxf(x, dpp<0x121>(x)); return x;
}
__device__ __forceinline__ float swap32_add(float a, float b) {
  auto r = __builtin_amdgcn_permlane32_swap(__float_as_uint(a), __float_as_uint(b), false, false);
  return __uint_as_float(r[0]) + __uint_as_float(r[1]);
}
__device__ __forceinline__ float swap16_add(float a, float b) {
  auto r = __builtin_amdgcn_permlane16_swap(__float_as_uint(a), __float_as_uint(b), false, false);
  return __uint_as_float(r[0]) + __uint_as_float(r[1]);
}
__device__ __forceinline__ float swap32_sum(float x) { return swap32_add(x, x); }
__device__ __forceinline__ float swap16_sum(float x) { return swap16_add(x, x); }
__device__ __forceinline__ float swap32_max(float x) {
  auto r = __builtin_amdgcn_permlane32_swap(__float_as_uint(x), __float_as_uint(x), false, false);
  return fmaxf(__uint_as_float(r[0]), __uint_as_float(r[1]));
}
__device__ __forceinline__ float swap16_max(float x) {
  auto r = __builtin_amdgcn_permlane16_swap(__float_as_uint(x), __float_as_uint(x), false, false);
  return fmaxf(__uint_as_float(r[0]), __uint_as_float(r[1]));
}
__device__ __forceinline__ void reduce8(const float (&v)[8], float& r0, float& r1) {
  const float a0 = swap32_add(v[0], v[1]), a1 = swap32_add(v[2], v[3]), a2 = swap32_add(v[4], v[5]), a3 = swap32_add(v[6], v[7]);
  r0 = row_sum(swap16_add(a0, a1));
  r1 = row_sum(swap16_add(a2, a3));
}
__device__ __forceinline__ float rdlane(float x, int l) { return __int_as_float(__builtin_amdgcn_readlane(__float_as_int(x), l)); }
#define RED8_LANE(i) (16 * ((((i) & 1) << 1) | (((i) >> 1) & 1)))

extern __shared__ __attribute__((aligned(16))) char smem[];
__device__ __forceinline__ u32x4 lds_r128(u32 off) { return *(const LDSP u32x4*)((LDSP char*)smem + off); }
__device__ __forceinline__ void lds_w128(u32 off, u32x4 v) { *(LDSP u32x4*)((LDSP char*)smem + off) = v; }
__device__ __forceinline__ f32x4 lds_rf4(u32 off) { return *(const LDSP f32x4*)((LDSP char*)smem + off); }
__device__ __forceinline__ void lds_wf4(u32 off, f32x4 v) { *(LDSP f32x4*)((LDSP char*)smem + off) = v; }
__device__ __forceinline__ float lds_rf(u32 off) { return *(const LDSP float*)((LDSP char*)smem + off); }
__device__ __forceinline__ void lds_wf(u32 off, float v) { *(LDSP float*)((LDSP char*)smem + off) = v; }
__device__ __forceinline__ u32x2 lds_r64(u32 off) { return *(const LDSP u32x2*)((LDSP char*)smem + off); }
__device__ __forceinline__ void lds_w64(u32 off, u32x2 v) { *(LDSP u32x2*)((LDSP char*)smem + off) = v; }
__device__ __forceinline__ void lds_w32(u32 off, u32 v) { *(LDSP u32*)((LDSP char*)smem + off) = v; }
__device__ __forceinline__ u32 lds_r32(u32 off) { return *(const LDSP u32*)((LDSP char*)smem + off); }
__device__ __forceinline__ u32 lds_base() { return (u32)(uintptr_t)(LDSP char*)smem; }
typedef short v4i16_t __attribute__((ext_vector_type(4)));
__device__ __forceinline__ u32x2 lds_tr(u32 off) {
  return __builtin_bit_cast(u32x2, __builtin_amdgcn_ds_read_tr16_b64_v4i16((LDSP v4i16_t*)((LDSP char*)smem + off)));
}
__device__ __forceinline__ u32x4 lds_tr2(u32 off, u32 step) {
  const u32x2 a = lds_tr(off), c = lds_tr(off + step);
  return u32x4{a[0], a[1], c[0], c[1]};
}

#define GLDS_ASM(NAME, BODY)                                                                             \
  __device__ __forceinline__ void NAME(const void* g, u32 voff, u32 lds) {                               \
    u32 sv;                                                                                              \
    asm volatile("s_mov_b32 %0, m0\n\ts_mov_b32 m0, %3\n\ts_nop 0\n\t" BODY "s_mov_b32 m0, %0"           \
                 : "=&s"(sv) : "v"(voff), "s"(g), "s"(lds) : "memory");                                  \
  }
GLDS_ASM(glds1, "global_load_lds_dwordx4 %1, %2\n\t")
GLDS_ASM(glds1_sc1, "global_load_lds_dwordx4 %1, %2 sc1\n\t")
GLDS_ASM(glds4_sc1, "global_load_lds_dwordx4 %1, %2 sc1\n\tglobal_load_lds_dwordx4 %1, %2 offset:1024 sc1\n\t"
                    "global_load_lds_dwordx4 %1, %2 offset:2048 sc1\n\tglobal_load_lds_dwordx4 %1, %2 offset:3072 sc1\n\t")
#undef GLDS_ASM

__device__ __forceinline__ u32 ld4_sc1(const void* p) { return __hip_atomic_load((GLBP u32*)p, RLX_AGENT); }
__device__ __forceinline__ void st4_sc1(void* p, u32 v) { __hip_atomic_store((GLBP u32*)p, v, RLX_AGENT); }
__device__ __forceinline__ void st8_sc1(void* p, u32 lo, u32 hi) {
  __hip_atomic_store((GLBP u64*)p, ((u64)hi << 32) | lo, RLX_AGENT);
}
typedef __amdgpu_buffer_rsrc_t rsrc_t;
__device__ __forceinline__ u32x4 bld128(rsrc_t rs, u32 off) { return __builtin_amdgcn_raw_buffer_load_b128(rs, off, 0, SC1); }
__device__ __forceinline__ u32x2 bld64(rsrc_t rs, u32 off) { return __builtin_amdgcn_raw_buffer_load_b64(rs, off, 0, SC1); }
__device__ __forceinline__ void bst128(rsrc_t rs, u32 off, u32x4 v) { __builtin_amdgcn_raw_buffer_store_b128(v, rs, off, 0, SC1); }
__device__ __forceinline__ void bst64(rsrc_t rs, u32 off, u32x2 v) { __builtin_amdgcn_raw_buffer_store_b64(v, rs, off, 0, SC1); }
#define ORDER_BEHIND_POLL() __builtin_amdgcn_fence(__ATOMIC_ACQUIRE, "wavefront")

__device__ __forceinline__ void poll_tags(const char* ws, u32 off, int n, u32 tag, int lane) {
  const char* p = ws + off + (lane < n ? lane : 0) * 4;
  _Pragma("unroll 1") for (int spin = 0; spin < SPIN_CAP; ++spin) {
    const u32 v = ld4_sc1(p);
    if (__all(v == tag)) break;
    __builtin_amdgcn_s_sleep(1);
  }
  ORDER_BEHIND_POLL();
}

#define STAMP(I)
#define STAMPE(I)

constexpr u32 P_WQ = 0, P_H = 0, P_WKV = 65536, P_STG = 81920, P_QX = 114688, P_PART = 147456, P_M1 = 149504;
constexpr u32 A_K = 0, A_V = 65536, A_X = 131072;
constexpr u32 L_ONES = 155648, L_MISC = 157696, L_MODI = 159744, LDS_BYTES = 163840;
constexpr u32 T_WO = 0, T_O = 65536, T_Y = 0, T_ST = 131072, T_RED = 0;
constexpr float QSCALE = 0.25f * LOG2E;
constexpr float BOUND2 = 64.f * 64.f;
#define EXP2(x) __builtin_amdgcn_exp2f(x)

__device__ __forceinline__ u32x4 ald128(const void* g, u32 voff) {
  u32x4 v; asm volatile("global_load_dwordx4 %0, %1, %2" : "=v"(v) : "v"(voff), "s"(g) : "memory"); return v;
}
__device__ __forceinline__ u32 ald16(const void* g, u32 voff) {
  u32 v; asm volatile("global_load_ushort %0, %1, %2" : "=v"(v) : "v"(voff), "s"(g) : "memory"); return v;
}
__device__ __forceinline__ u32 ald32_sc1(const void* g, u32 voff) {
  u32 v; asm volatile("global_load_dword %0, %1, %2 sc1" : "=v"(v) : "v"(voff), "s"(g) : "memory"); return v;
}
__device__ __forceinline__ void glds1_v(const void* p, u32 lds) {
  u32 sv;
  asm volatile("s_mov_b32 %0, m0\n\ts_mov_b32 m0, %2\n\ts_nop 0\n\tglobal_load_lds_dwordx4 %1, off\n\ts_mov_b32 m0, %0"
               : "=&s"(sv) : "v"(p), "s"(lds) : "memory");
}

#define HALF(Sc, Sn, Kn, Qn, Oc, V0, V1, CIN)                                                          \
  do {                                                                                                 \
    u32x4 p0_, p1_;                                                                                    \
    Sc[0] = EXP2(Sc[0]); Sc[1] = EXP2(Sc[1]); Sc[2] = EXP2(Sc[2]); Sc[3] = EXP2(Sc[3]);                \
    p0_[0] = pk2(Sc[0], Sc[1]); p0_[1] = pk2(Sc[2], Sc[3]); PIN(p0_[0]); PIN(p0_[1]); SBAR();          \
    Sn = MFMA32(Kn, Qn, CIN); SBAR();                                                                  \
    Sc[4] = EXP2(Sc[4]); Sc[5] = EXP2(Sc[5]); Sc[6] = EXP2(Sc[6]); Sc[7] = EXP2(Sc[7]);                \
    p0_[2] = pk2(Sc[4], Sc[5]); p0_[3] = pk2(Sc[6], Sc[7]); PIN(p0_); SBAR();                          \
    Oc = MFMA32(V0, p0_, Oc); SBAR();                                                                  \
    Sc[8] = EXP2(Sc[8]); Sc[9] = EXP2(Sc[9]); Sc[10] = EXP2(Sc[10]); Sc[11] = EXP2(Sc[11]);            \
    p1_[0] = pk2(Sc[8], Sc[9]); p1_[1] = pk2(Sc[10], Sc[11]); PIN(p1_[0]); PIN(p1_[1]); SBAR();        \
    Sc[12] = EXP2(Sc[12]); Sc[13] = EXP2(Sc[13]); Sc[14] = EXP2(Sc[14]); Sc[15] = EXP2(Sc[15]);        \
    p1_[2] = pk2(Sc[12], Sc[13]); p1_[3] = pk2(Sc[14], Sc[15]); PIN(p1_); SBAR();                      \
    Oc = MFMA32(V1, p1_, Oc); SBAR();                                                                  \
  } while (0)

__global__ __launch_bounds__(512, 2) void fused_block(const u16* __restrict__ x, const u16* __restrict__ wq, const u16* __restrict__ wk,
                                                      const u16* __restrict__ wv, const u16* __restrict__ wo, const u16* __restrict__ wg,
                                                      const u16* __restrict__ wu, const u16* __restrict__ wd, const u16* __restrict__ cvec,
                                                      const u16* __restrict__ wmod, char* __restrict__ ws, u16* __restrict__ out) {
  const int tid = threadIdx.x, lane = tid & 63, b = blockIdx.x;
  const int wave = __builtin_amdgcn_readfirstlane(tid >> 6);
  const int r = lane & 31, hh = lane >> 5, g = lane >> 4, li = lane & 15;
  const int xcd = b & 7, idx = b >> 3;
  const int tg = xcd * 4 + (idx >> 3), hg = idx & 7;
  const u32 lb = lds_base(), vo = lane * 16;
  const rsrc_t rs = __builtin_amdgcn_make_buffer_rsrc((void*)ws, 0, WS_END, 0x00020000);
  STAMP(0);

  const int colA = idx < 16 ? 64 * (xcd + 8 * (idx >> 3)) + 8 * (idx & 7) : 1024 + 64 * (xcd + 8 * ((idx - 16) >> 2)) + 16 * (idx & 3);
  u32 ev = ald32_sc1(ws + WS_EPOCH, 0);
  u32 cv = ald16(cvec, tid * 2);
  u32x4 wA = ald128(wmod, tid * 6144 + colA * 2);
  u32x4 wB = ald128(wmod, idx < 16 ? 0 : tid * 6144 + colA * 2 + 16);
  u32x4 xr[8];
#pragma unroll
  for (int i = 0; i < 8; ++i) xr[i] = ald128(x + (64 * tg + 8 * wave + i) * DM, vo);
  {
    const u32 vq = (lane >> 3) * 1024 + hg * 128 + (((lane & 7) ^ (((lane >> 4) & 1) << 2)) * 16);
#pragma unroll
    for (int i = 0; i < 8; ++i) glds1(wq + (64 * wave + 8 * i) * DM, vq, lb + P_WQ + (64 * wave + 8 * i) * 128);
    const u16* wkv = (hg & 1) ? wv : wk;
#pragma unroll
    for (int i = 0; i < 2; ++i) glds1(wkv + wave * 1024 + i * 512, vo, lb + P_WKV + wave * 2048 + i * 1024);
  }
  lds_w32(L_ONES + tid * 4, 0x3f803f80u);
  asm volatile("s_waitcnt vmcnt(18)" : "+v"(ev), "+v"(cv), "+v"(wA), "+v"(wB)::"memory");
  const u32 tag = __builtin_amdgcn_readfirstlane(ev) + 1;
  {
    const float f = bf2f(cv), sl = rbf(f / (1.f + __expf(-f)));
    float a[8], r0, r1;
    const int sub = ((g & 1) << 1) | (g >> 1);
#pragma unroll
    for (int i = 0; i < 4; ++i) { a[2 * i] = sl * lo2f(wA[i]); a[2 * i + 1] = sl * hi2f(wA[i]); }
    reduce8(a, r0, r1);
    if (li == 0) { lds_wf(P_PART + (wave * 8 + sub) * 4, r0); lds_wf(P_PART + (wave * 8 + 4 + sub) * 4, r1); }
    if (idx >= 16) {
#pragma unroll
      for (int i = 0; i < 4; ++i) { a[2 * i] = sl * lo2f(wB[i]); a[2 * i + 1] = sl * hi2f(wB[i]); }
      reduce8(a, r0, r1);
      if (li == 0) { lds_wf(P_PART + 256 + (wave * 8 + sub) * 4, r0); lds_wf(P_PART + 256 + (wave * 8 + 4 + sub) * 4, r1); }
    }
  }
  BAR();
  STAMP(1);
  if (wave == 0) {
    const int jl = lane & 15;
    float a = 0;
#pragma unroll
    for (int w = 0; w < 8; ++w) a += lds_rf(P_PART + (jl >> 3) * 256 + (w * 8 + (jl & 7)) * 4);
    a = rbf(a);
    const int col = colA + jl, part = col >> 9;
    if (part == 1 || part == 4) a = rbf(1.f + a);
    const float an = dpp<0xB1>(a);
    if (lane < (idx < 16 ? 8 : 16) && !(lane & 1)) st8_sc1(ws + WS_MODG + (col >> 1) * 8, tag, pk2(a, an));
  }
  float xh[8][8];
  {
    asm volatile("s_waitcnt vmcnt(10)"
                 : "+v"(xr[0]), "+v"(xr[1]), "+v"(xr[2]), "+v"(xr[3]), "+v"(xr[4]), "+v"(xr[5]), "+v"(xr[6]), "+v"(xr[7])::"memory");
    float s[8], r0, r1;
#pragma unroll
    for (int i = 0; i < 8; ++i) {
#pragma unroll
      for (int j = 0; j < 4; ++j) { xh[i][2 * j] = lo2f(xr[i][j]); xh[i][2 * j + 1] = hi2f(xr[i][j]); }
      s[i] = ((xh[i][0] + xh[i][1]) + (xh[i][2] + xh[i][3])) + ((xh[i][4] + xh[i][5]) + (xh[i][6] + xh[i][7]));
    }
    reduce8(s, r0, r1);
#pragma unroll
    for (int i = 0; i < 8; ++i) {
      const float m = rdlane(i < 4 ? r0 : r1, RED8_LANE(i)) * (1.f / DM);
      float q = 0;
#pragma unroll
      for (int j = 0; j < 8; ++j) { xh[i][j] -= m; q += xh[i][j] * xh[i][j]; }
      s[i] = q;
    }
    reduce8(s, r0, r1);
    r0 = rsqrtf(r0 * (1.f / DM) + 1e-5f); r1 = rsqrtf(r1 * (1.f / DM) + 1e-5f);
#pragma unroll
    for (int i = 0; i < 8; ++i) {
      const float rstd = rdlane(i < 4 ? r0 : r1, RED8_LANE(i));
#pragma unroll
      for (int j = 0; j < 8; ++j) xh[i][j] *= rstd;
    }
  }
  STAMP(2);
  WAITV_BAR(0);
  STAMP(3);
  const int u = wave * 256 + xcd * 32 + idx;
  if (wave < 6 && u < 1472) {
    const int R = lane >> 3, cp = lane & 7;
    const char* src; u32 ld;
    if (u < 128)       { src = (const char*)wo + (32 * (u >> 3) + R) * 1024 + (u & 7) * 128 + cp * 16; ld = 1024; }
    else if (u < 1024) { const int e = u - 128, kb = e / 56, ab = e - kb * 56;
                         src = (const char*)(cp < 4 ? wg : wu) + (32 * kb + R) * (FF * 2) + ab * 64 + (cp & 3) * 16; ld = FF * 2; }
    else               { const int e = u - 1024; src = (const char*)wd + (32 * (e >> 3) + R) * 1024 + (e & 7) * 128 + cp * 16; ld = 1024; }
#pragma unroll
    for (int i = 0; i < 4; ++i) glds1_v(src + i * 8 * ld, lb + P_STG + wave * 4096 + i * 1024);
  }
  const int th = wave & 1, hp = (wave >> 1) & 1, kh = wave >> 2;
  const int tq_ = li >> 2, tp_ = lane & 3;
  u32x4 wqr[16];
  {
    const u32 a0 = P_WQ + kh * 32768 + (8 * (g >> 1) + tq_) * 128 + (((4 * hp + 2 * (g & 1) + (tp_ >> 1)) ^ ((tq_ >> 1) << 2)) * 16) + 8 * (tp_ & 1);
#pragma unroll
    for (int i = 0; i < 16; ++i) wqr[i] = lds_tr2(a0 + i * 2048, 512);
  }
  if (wave == 6) {
    _Pragma("unroll 1") for (int spin = 0; spin < SPIN_CAP; ++spin) {
      u32x4 q[4];
#pragma unroll
      for (int i = 0; i < 4; ++i) q[i] = bld128(rs, WS_MODG + i * 1024 + vo);
      bool ok = true;
#pragma unroll
      for (int i = 0; i < 4; ++i) { ok &= (q[i][0] == tag) & (q[i][2] == tag); lds_w64(P_M1 + i * 512 + lane * 8, u32x2{q[i][1], q[i][3]}); }
      if (__all(ok)) break;
      __builtin_amdgcn_s_sleep(1);
    }
  }
  STAMP(4);
  BAR();
  STAMP(5);
  {
    const u32x4 sa = lds_r128(P_M1 + vo), ca = lds_r128(P_M1 + 1024 + vo);
    float sf[8], cf[8];
#pragma unroll
    for (int j = 0; j < 4; ++j) { sf[2 * j] = lo2f(sa[j]); sf[2 * j + 1] = hi2f(sa[j]); cf[2 * j] = lo2f(ca[j]); cf[2 * j + 1] = hi2f(ca[j]); }
#pragma unroll
    for (int i = 0; i < 8; ++i) {
      const int rho = 8 * wave + i, rr = rho & 31;
      u32x4 hv;
#pragma unroll
      for (int j = 0; j < 4; ++j) hv[j] = pk2(xh[i][2 * j] * cf[2 * j] + sf[2 * j], xh[i][2 * j + 1] * cf[2 * j + 1] + sf[2 * j + 1]);
      lds_w128(P_H + (rho >> 5) * 32768 + (lane >> 1) * 1024 + (lane & 1) * 512 + ((rr ^ li) * 16), hv);
    }
  }
  BAR();
  STAMP(6);
  float kn2 = 0;
  if (wave == 7) {
    const int q = hg >> 1, m = hg & 1;
    const int rr = (q & 1) * 16 + li;
    f32x4 d0 = {0, 0, 0, 0}, d1 = {0, 0, 0, 0};
    const u32 wa = P_WKV + (8 * g + tq_) * 32 + 8 * tp_;
    const u32 ha = P_H + (q >> 1) * 32768 + (g >> 1) * 1024 + (g & 1) * 512;
    u32x4 wf[16], hf[16];
#pragma unroll
    for (int t = 0; t < 16; ++t) {
      wf[t] = lds_tr2(wa + t * 1024, 128);
      hf[t] = lds_r128(ha + t * 2048 + ((rr ^ ((4 * t + g) & 15)) * 16));
    }
    if (m == 0) {
#pragma unroll
      for (int t = 0; t < 16; t += 2) { d0 = MFMA16(wf[t], hf[t], d0); d1 = MFMA16(wf[t + 1], hf[t + 1], d1); }
    } else {
#pragma unroll
      for (int t = 0; t < 16; t += 2) { d0 = MFMA16(hf[t], wf[t], d0); d1 = MFMA16(hf[t + 1], wf[t + 1], d1); }
    }
    d0 += d1;
    const u32x2 pkd = {pk2(d0[0], d0[1]), pk2(d0[2], d0[3])};
    const int T = 2 * tg + (q >> 1);
    if (m == 0) {
      st8_sc1(ws + WS_KF + T * 1024 + (g & 1) * 512 + rr * 16 + (g >> 1) * 8, pkd[0], pkd[1]);
      const float k0 = lo2f(pkd[0]), k1 = hi2f(pkd[0]), k2 = lo2f(pkd[1]), k3 = hi2f(pkd[1]);
      kn2 = row_max(swap32_sum(swap16_sum(k0 * k0 + k1 * k1 + k2 * k2 + k3 * k3)));
    } else {
      st8_sc1(ws + WS_VF + T * 1024 + (q & 1) * 512 + (g & 1) * 256 + li * 16 + (g >> 1) * 8, pkd[0], pkd[1]);
    }
  }
  f32x16 qa = {};
#pragma unroll
  for (int i = 0; i < 16; ++i) {
    const int ks = 16 * kh + i, cch = 2 * ks + hh;
    const u32x4 hf = lds_r128(P_H + th * 32768 + ks * 1024 + hh * 512 + ((r ^ (cch & 15)) * 16));
    qa = MFMA32(wqr[i], hf, qa);
  }
  if (wave == 7) {
    WAITV(0);
    if (lane == 0) st8_sc1(ws + WS_KVFL + b * 8, tag, __float_as_uint(kn2));
  }
#pragma unroll
  for (int i = 0; i < 4; ++i) lds_wf4(P_QX + wave * 4096 + i * 1024 + vo, f32x4{qa[4 * i], qa[4 * i + 1], qa[4 * i + 2], qa[4 * i + 3]});
  STAMP(7);
  BAR();
  STAMP(8);
  u32x4 Qa, Qb; float qn2;
  {
    float t[16];
#pragma unroll
    for (int i = 0; i < 4; ++i) {
      const f32x4 o = lds_rf4(P_QX + (wave ^ 4) * 4096 + i * 1024 + vo);
#pragma unroll
      for (int j = 0; j < 4; ++j) t[4 * i + j] = (qa[4 * i + j] + o[j]) * QSCALE;
    }
#pragma unroll
    for (int j = 0; j < 4; ++j) { Qa[j] = pk2(t[2 * j], t[2 * j + 1]); Qb[j] = pk2(t[8 + 2 * j], t[9 + 2 * j]); }
    float na = 0, nb = 0;
#pragma unroll
    for (int j = 0; j < 4; ++j) {
      na += lo2f(Qa[j]) * lo2f(Qa[j]) + hi2f(Qa[j]) * hi2f(Qa[j]);
      nb += lo2f(Qb[j]) * lo2f(Qb[j]) + hi2f(Qb[j]) * hi2f(Qb[j]);
    }
    qn2 = fmaxf(swap32_sum(na), swap32_sum(nb));
  }
  {
    u32x4 q;
    _Pragma("unroll 1") for (int spin = 0; spin < SPIN_CAP; ++spin) {
      q = bld128(rs, WS_MODG + 4096 + tid * 16);
      if (__all((q[0] == tag) & (q[2] == tag))) break;
      __builtin_amdgcn_s_sleep(1);
    }
    const int n = (tid * 4) & 511;
    lds_w64(L_MODI + (tid >> 7) * 1024 + (n >> 5) * 64 + ((n >> 2) & 3) * 16 + ((n >> 4) & 1) * 8, u32x2{q[1], q[3]});
  }
  STAMP(9);
  if (wave < 6 && u < 1472) {
    WAITV(0);
    const u32 sb = P_STG + wave * 4096 + tq_ * 128 + 8 * tp_;
    if (u < 128) {
#pragma unroll
      for (int s4 = 0; s4 < 4; ++s4)
        bst128(rs, WS_WOT + ((4 * (u & 7) + s4) * 16 + (u >> 3)) * 1024 + vo, lds_tr2(sb + (16 * (g >> 1) + 4 * (g & 1)) * 128 + 32 * s4, 1024));
    } else if (u < 1024) {
      const int e = u - 128, kb = e / 56, ab = e - kb * 56;
#pragma unroll
      for (int f = 0; f < 4; ++f)
        bst128(rs, WS_W1T + ((2 * ab + (f >> 1)) * 32 + 2 * kb + (f & 1)) * 1024 + vo,
               lds_tr2(sb + (8 * (f & 1) + 4 * (g >> 1)) * 128 + (g & 1) * 64 + 32 * (f >> 1), 2048));
    } else {
      const int e = u - 1024;
#pragma unroll
      for (int f = 0; f < 4; ++f)
        bst128(rs, WS_WDT + ((2 * (e & 7) + (f >> 1)) * 112 + 2 * (e >> 3) + (f & 1)) * 1024 + vo,
               lds_tr2(sb + (16 * (f & 1) + 4 * (g >> 1)) * 128 + 64 * (f >> 1) + 32 * (g & 1), 1024));
    }
  }
  STAMP(10);
  if (wave == 6) {
    float km = 0;
    _Pragma("unroll 1") for (int spin = 0; spin < SPIN_CAP; ++spin) {
      const u32x4 q0 = bld128(rs, WS_KVFL + vo), q1 = bld128(rs, WS_KVFL + 1024 + vo);
      km = fmaxf(fmaxf(__uint_as_float(q0[1]), __uint_as_float(q0[3])), fmaxf(__uint_as_float(q1[1]), __uint_as_float(q1[3])));
      if (__all((q0[0] == tag) & (q0[2] == tag) & (q1[0] == tag) & (q1[2] == tag))) break;
      __builtin_amdgcn_s_sleep(1);
    }
    km = swap32_max(swap16_max(row_max(km)));
    if (lane == 0) {
      lds_wf(L_MISC, km);
      if (b == 0) st4_sc1(ws + WS_EPOCH, tag);
    }
    ORDER_BEHIND_POLL();
  }
  BAR();
  STAMP(11);
  {
    glds1_sc1(ws + WS_KF + wave * 1024, vo, lb + A_K + wave * 1024);
    glds1_sc1(ws + WS_VF + wave * 1024, vo, lb + A_V + wave * 1024);
    glds1_sc1(ws + WS_KF + (32 + wave) * 1024, vo, lb + A_K + (32 + wave) * 1024);
    glds1_sc1(ws + WS_VF + (32 + wave) * 1024, vo, lb + A_V + (32 + wave) * 1024);
#pragma unroll
    for (int c = 1; c < 4; ++c) {
      const int t0 = 8 * c + wave, t1 = 32 + t0;
      glds1_sc1(ws + WS_KF + t0 * 1024, vo, lb + A_K + t0 * 1024);
      glds1_sc1(ws + WS_VF + t0 * 1024, vo, lb + A_V + t0 * 1024);
      glds1_sc1(ws + WS_KF + t1 * 1024, vo, lb + A_K + t1 * 1024);
      glds1_sc1(ws + WS_VF + t1 * 1024, vo, lb + A_V + t1 * 1024);
    }
  }
  const float km2 = lds_rf(L_MISC);
  const bool safe = !__any(!(qn2 * km2 <= BOUND2));
  WAITV_BAR(12);
  STAMP(12);

  f32x16 Oa = {}, Ob = {};
  const u32 kadr = A_K + kh * 32768 + vo;
  u32 vadr = (r < 16) ? (A_V + kh * 32768 + hh * 256 + r * 16) : (L_ONES + ((r >= 20 && r < 28) ? 64 : 0));
  const u32 vstep = (r < 16) ? 1024u : 0u;
  if (safe) {
    f32x16 Sa, Sb;
    const f32x16 zero = {};
    u32x4 Kc = lds_r128(kadr);
    Sa = MFMA32(Kc, Qa, zero);
    u32x4 V0 = lds_r128(vadr), V1 = lds_r128(vadr + 512);
#pragma unroll 1
    for (int t = 0; t < 32; t += 2) {
      if (t == 6) {
        WAITV_BAR(0);
        if (tid == 0) st4_sc1(ws + WS_PREP + b * 4, tag);
      }
      const u32 ka = kadr + t * 1024;
      u32x4 Kn = lds_r128(ka + 1024);
      u32x4 W0 = lds_r128(vadr + 1024), W1 = lds_r128(vadr + 1536);
      SBAR();
      HALF(Sa, Sb, Kc, Qb, Oa, V0, V1, zero);
      HALF(Sb, Sa, Kn, Qa, Ob, V0, V1, zero);
      vadr += 2 * vstep;
      Kc = lds_r128(ka + 2048);
      V0 = lds_r128(vadr); V1 = lds_r128(vadr + 512);
      SBAR();
      HALF(Sa, Sb, Kn, Qb, Oa, W0, W1, zero);
      HALF(Sb, Sa, Kc, Qa, Ob, W0, W1, zero);
    }
  } else {
    WAITV_BAR(0);
    if (tid == 0) st4_sc1(ws + WS_PREP + b * 4, tag);
#pragma unroll 1
    for (int x2 = 0; x2 < 2; ++x2) {
      const u32x4 Q = x2 ? Qb : Qa;
      float m = -3.0e38f;
      const f32x16 zero = {};
#pragma unroll 1
      for (int t = 0; t < 64; ++t) {
        const f32x16 s = MFMA32(lds_r128(A_K + t * 1024 + vo), Q, zero);
#pragma unroll
        for (int j = 0; j < 16; ++j) m = fmaxf(m, s[j]);
      }
      m = swap32_max(m);
      f32x16 cm;
#pragma unroll
      for (int j = 0; j < 16; ++j) cm[j] = -m;
      f32x16 O = {};
      u32 va = vadr;
#pragma unroll 1
      for (int t = 0; t < 32; ++t) {
        f32x16 s = MFMA32(lds_r128(kadr + t * 1024), Q, cm);
        u32x4 p0, p1;
#pragma unroll
        for (int j = 0; j < 4; ++j) { p0[j] = pk2(EXP2(s[2 * j]), EXP2(s[2 * j + 1])); p1[j] = pk2(EXP2(s[8 + 2 * j]), EXP2(s[9 + 2 * j])); }
        O = MFMA32(lds_r128(va), p0, O);
        O = MFMA32(lds_r128(va + 512), p1, O);
        va += vstep;
      }
      if (x2) Ob = O; else Oa = O;
    }
  }
  STAMP(13);
  if (wave == 0) {
    _Pragma("unroll 1") for (int spin = 0; spin < SPIN_CAP; ++spin) {
      const u32x4 q = bld128(rs, WS_PREP + vo);
      if (__all((q[0] == tag) & (q[1] == tag) & (q[2] == tag) & (q[3] == tag))) break;
      __builtin_amdgcn_s_sleep(1);
    }
    ORDER_BEHIND_POLL();
  }
  {
    const u32 o = A_X + wave * 3072 + vo;
    if (kh == 0) {
      lds_wf4(o, f32x4{Ob[0], Ob[1], Ob[2], Ob[3]}); lds_wf4(o + 1024, f32x4{Ob[4], Ob[5], Ob[6], Ob[7]}); lds_wf(A_X + 2048 + wave * 3072 + lane * 4, Ob[8]);
    } else {
      lds_wf4(o, f32x4{Oa[0], Oa[1], Oa[2], Oa[3]}); lds_wf4(o + 1024, f32x4{Oa[4], Oa[5], Oa[6], Oa[7]}); lds_wf(A_X + 2048 + wave * 3072 + lane * 4, Oa[8]);
    }
  }
  BAR();
  STAMP(14);
  {
    const u32 o = A_X + (wave ^ 4) * 3072 + vo;
    const f32x4 a = lds_rf4(o), c = lds_rf4(o + 1024);
    const float l2 = lds_rf(A_X + 2048 + (wave ^ 4) * 3072 + lane * 4);
    float t[8], l;
    if (kh == 0) {
#pragma unroll
      for (int j = 0; j < 4; ++j) { t[j] = Oa[j] + a[j]; t[4 + j] = Oa[4 + j] + c[j]; }
      l = Oa[8] + l2;
    } else {
#pragma unroll
      for (int j = 0; j < 4; ++j) { t[j] = Ob[j] + a[j]; t[4 + j] = Ob[4 + j] + c[j]; }
      l = Ob[8] + l2;
    }
    const float il = 1.f / l;
    const u32x4 ov = {pk2(t[0] * il, t[1] * il), pk2(t[2] * il, t[3] * il), pk2(t[4] * il, t[5] * il), pk2(t[6] * il, t[7] * il)};
    bst128(rs, WS_OF + ((2 * tg + th) * 32 + 4 * hg + 2 * hp + kh) * 1024 + vo, ov);
  }
  const int ng = hg, tq = wave & 3, nh = wave >> 2;
  const int mb = 2 * ng + nh;
  const int tok = 64 * tg + 16 * tq + li;
  asm volatile("" ::: "memory");
#pragma unroll
  for (int i = 0; i < 8; ++i) glds1_sc1(ws + WS_WOT + (ng * 64 + wave * 8 + i) * 1024, vo, lb + T_WO + (wave * 8 + i) * 1024);
  WAITV_BAR(8);
  if (tid == 0) st4_sc1(ws + WS_OFL + tg * 128 + hg * 4, tag);
  STAMP(15);
  const u32x2 xr0 = *(const u32x2*)(x + tok * DM + 32 * mb + 4 * g), xr1 = *(const u32x2*)(x + tok * DM + 32 * mb + 16 + 4 * g);
  if (wave == 1) poll_tags(ws, WS_OFL + tg * 128, 8, tag, lane);
  BAR();
  STAMP(16);
#pragma unroll
  for (int i = 0; i < 8; ++i) glds1_sc1(ws + WS_OF + (2 * tg * 32 + wave * 8 + i) * 1024, vo, lb + T_O + (wave * 8 + i) * 1024);
  WAITV_BAR(0);
  STAMP(17);
  u32 x2p[4];
  {
    f32x4 a0 = {0, 0, 0, 0}, a1 = {0, 0, 0, 0};
    const u32 wa = T_WO + 2 * nh * 16384 + vo;
    const u32 oa = T_O + (tq >> 1) * 32768 + (g >> 1) * 1024 + (g & 1) * 512 + (16 * (tq & 1) + li) * 16;
#pragma unroll
    for (int t = 0; t < 16; ++t) {
      const u32x4 of = lds_r128(oa + t * 2048);
      a0 = MFMA16(lds_r128(wa + t * 1024), of, a0);
      a1 = MFMA16(lds_r128(wa + 16384 + t * 1024), of, a1);
    }
    const u32x4 ga = lds_r128(L_MODI + mb * 64 + g * 16), cb = lds_r128(L_MODI + 2048 + mb * 64 + g * 16);
    x2p[0] = pk2(lo2f(xr0[0]) + lo2f(ga[0]) * a0[0], hi2f(xr0[0]) + hi2f(ga[0]) * a0[1]);
    x2p[1] = pk2(lo2f(xr0[1]) + lo2f(ga[1]) * a0[2], hi2f(xr0[1]) + hi2f(ga[1]) * a0[3]);
    x2p[2] = pk2(lo2f(xr1[0]) + lo2f(ga[2]) * a1[0], hi2f(xr1[0]) + hi2f(ga[2]) * a1[1]);
    x2p[3] = pk2(lo2f(xr1[1]) + lo2f(ga[3]) * a1[2], hi2f(xr1[1]) + hi2f(ga[3]) * a1[3]);
    float xv[8];
    u32x4 y2;
#pragma unroll
    for (int j = 0; j < 4; ++j) {
      xv[2 * j] = lo2f(x2p[j]); xv[2 * j + 1] = hi2f(x2p[j]);
      y2[j] = pk2(xv[2 * j] * lo2f(cb[j]), xv[2 * j + 1] * hi2f(cb[j]));
    }
    bst128(rs, WS_Y2F + ((2 * tg + (tq >> 1)) * 32 + 2 * mb + (g >> 1)) * 1024 + (g & 1) * 512 + (16 * (tq & 1) + li) * 16, y2);
    float s = ((xv[0] + xv[1]) + (xv[2] + xv[3])) + ((xv[4] + xv[5]) + (xv[6] + xv[7]));
    const float m32 = swap32_sum(swap16_sum(s)) * (1.f / 32.f);
    float q = 0;
#pragma unroll
    for (int j = 0; j < 8; ++j) { const float d = xv[j] - m32; q += d * d; }
    q = swap32_sum(swap16_sum(q));
    if (g == 0) bst64(rs, WS_ST + ((4 * tg + tq) * 16 + mb) * 128 + li * 8, u32x2{__float_as_uint(m32), __float_as_uint(q)});
  }
  STAMP(18);
  const int G = (xcd >> 2) * 8 + (idx & 7), cs = (xcd & 3) * 4 + (idx >> 3);
  const int at = 7 * cs + wave;
  u32x4 w[32];
  asm volatile("" ::: "memory");
  if (wave < 7) {
#pragma unroll
    for (int i = 0; i < 32; ++i) w[i] = bld128(rs, WS_W1T + (at * 32 + i) * 1024 + vo);
    WAITV_BAR(32);
  } else {
    WAITV_BAR(0);
  }
  if (tid == 448) st4_sc1(ws + WS_YFL + (tg >> 1) * 128 + ((tg & 1) * 8 + ng) * 4, tag);
  STAMP(19);
  if (wave == 7) poll_tags(ws, WS_YFL + G * 128, 16, tag, lane);
  BAR();
  STAMP(20);
#pragma unroll
  for (int tt = 0; tt < 4; ++tt) glds4_sc1(ws + WS_Y2F + ((4 * G + tt) * 32 + 4 * wave) * 1024, vo, lb + T_Y + tt * 32768 + wave * 4096);
  if (wave == 7) {
    u32x4 p[16];
#pragma unroll
    for (int i = 0; i < 16; ++i) p[i] = bld128(rs, WS_ST + ((8 * G + (lane >> 3)) * 16 + i) * 128 + (lane & 7) * 16);
    float m0 = 0, m1 = 0, q0 = 0, q1 = 0;
#pragma unroll
    for (int i = 0; i < 16; ++i) {
      m0 += __uint_as_float(p[i][0]); q0 += __uint_as_float(p[i][1]); m1 += __uint_as_float(p[i][2]); q1 += __uint_as_float(p[i][3]);
    }
    m0 *= (1.f / 16.f); m1 *= (1.f / 16.f);
#pragma unroll
    for (int i = 0; i < 16; ++i) {
      const float d0 = __uint_as_float(p[i][0]) - m0, d1 = __uint_as_float(p[i][2]) - m1;
      q0 += 32.f * d0 * d0; q1 += 32.f * d1 * d1;
    }
    const float r0 = rsqrtf(q0 * (1.f / DM) + 1e-5f), r1 = rsqrtf(q1 * (1.f / DM) + 1e-5f);
    lds_wf4(T_ST + lane * 16, f32x4{r0, m0 * r0, r1, m1 * r1});
  }
  float ug[8], uu[8], sg[8], su[8];
  if (wave < 7) {
    f32x16 us = {};
    const u32 ma = L_MODI + ((lane & 1) ? 1024 : 2048) + hh * 16;
#pragma unroll
    for (int i = 0; i < 32; ++i) us = MFMA32(w[i], lds_r128(ma + i * 32), us);
#pragma unroll
    for (int j = 0; j < 8; ++j) {
      ug[j] = dpp<0xA0>(us[j]); uu[j] = dpp<0xA0>(us[8 + j]);
      sg[j] = -LOG2E * dpp<0xF5>(us[j]); su[j] = dpp<0xF5>(us[8 + j]);
    }
  }
  STAMP(21);
  WAITV_BAR(0);
  STAMP(22);
  if (wave < 7) {
#pragma unroll 1
    for (int tt = 0; tt < 4; ++tt) {
      f32x16 acc = {};
#pragma unroll
      for (int i = 0; i < 32; ++i) acc = MFMA32(w[i], lds_r128(T_Y + tt * 32768 + i * 1024 + vo), acc);
      const u32x2 st = lds_r64(T_ST + (tt * 32 + r) * 8);
      const float rsd = __uint_as_float(st[0]), mr = __uint_as_float(st[1]);
      const float rg = -LOG2E * rsd, mg = LOG2E * mr;
      float hv[8];
#pragma unroll
      for (int j = 0; j < 8; ++j) {
        const float gt = rg * acc[j] + (mg * ug[j] + sg[j]);
        const float up = rsd * acc[8 + j] + (su[j] - mr * uu[j]);
        hv[j] = up * __builtin_amdgcn_rcpf(1.f + EXP2(gt));
      }
      bst128(rs, WS_HF + ((4 * G + tt) * 112 + at) * 1024 + vo,
             u32x4{pk2(hv[0], hv[1]), pk2(hv[2], hv[3]), pk2(hv[4], hv[5]), pk2(hv[6], hv[7])});
    }
  }
  STAMP(23);
  const int kq = wave & 3;
  u32x4 wdr[28];
  asm volatile("" ::: "memory");
#pragma unroll
  for (int i = 0; i < 28; ++i) wdr[i] = bld128(rs, WS_WDT + ((2 * ng + nh) * 112 + 28 * kq + i) * 1024 + vo);
  WAITV_BAR(28);
  if (tid == 0) st4_sc1(ws + WS_HFL + G * 128 + cs * 4, tag);
  STAMP(24);
  if (wave == 1) poll_tags(ws, WS_HFL + (tg >> 1) * 128, 16, tag, lane);
  BAR();
  STAMP(25);
  {
    const char* gsrc = ws + WS_HF + ((2 * tg + nh) * 112 + 28 * kq) * 1024;
    const u32 ldst = lb + wave * 4096;
    glds4_sc1(gsrc, vo, ldst);
    glds4_sc1(gsrc + 4096, vo, ldst + 32768);
    glds4_sc1(gsrc + 8192, vo, ldst + 65536);
    f32x16 c0 = {}, c1 = {};
    const u32 ba = kq * 4096 + vo;
#pragma unroll
    for (int c = 0; c < 7; ++c) {
      if (c < 5) WAITV_BAR(8); else if (c == 5) WAITV_BAR(4); else WAITV_BAR(0);
      if (c == 0) STAMP(26);
      if (c + 3 < 7) glds4_sc1(gsrc + (c + 3) * 4096, vo, ldst + ((c + 3) & 3) * 32768);
      const u32 so = (c & 3) * 32768 + ba;
#pragma unroll
      for (int i = 0; i < 4; ++i) {
        c0 = MFMA32(wdr[4 * c + i], lds_r128(so + i * 1024), c0);
        c1 = MFMA32(wdr[4 * c + i], lds_r128(so + 16384 + i * 1024), c1);
      }
    }
    STAMP(27);
    BAR();
    const u32 ro = T_RED + wave * 8192 + hh * 512 + r * 16;
#pragma unroll
    for (int q = 0; q < 4; ++q) {
      lds_wf4(ro + q * 1024, f32x4{c0[4 * q], c0[4 * q + 1], c0[4 * q + 2], c0[4 * q + 3]});
      lds_wf4(ro + 4096 + q * 1024, f32x4{c1[4 * q], c1[4 * q + 1], c1[4 * q + 2], c1[4 * q + 3]});
    }
  }
  BAR();
  {
    f32x4 a0 = {0, 0, 0, 0}, a1 = {0, 0, 0, 0};
    const u32 ro = T_RED + nh * 32768 + (tq >> 1) * 4096 + g * 512 + (16 * (tq & 1) + li) * 16;
#pragma unroll
    for (int k4 = 0; k4 < 4; ++k4) { a0 += lds_rf4(ro + k4 * 8192); a1 += lds_rf4(ro + k4 * 8192 + 2048); }
    const u32x4 gb = lds_r128(L_MODI + 3072 + mb * 64 + g * 16);
    const u32 p0 = pk2(lo2f(x2p[0]) + lo2f(gb[0]) * a0[0], hi2f(x2p[0]) + hi2f(gb[0]) * a0[1]);
    const u32 p1 = pk2(lo2f(x2p[1]) + lo2f(gb[1]) * a0[2], hi2f(x2p[1]) + hi2f(gb[1]) * a0[3]);
    const u32 p2 = pk2(lo2f(x2p[2]) + lo2f(gb[2]) * a1[0], hi2f(x2p[2]) + hi2f(gb[2]) * a1[1]);
    const u32 p3 = pk2(lo2f(x2p[3]) + lo2f(gb[3]) * a1[2], hi2f(x2p[3]) + hi2f(gb[3]) * a1[3]);
    const auto s0 = __builtin_amdgcn_permlane16_swap(p0, p2, false, false);
    const auto s1 = __builtin_amdgcn_permlane16_swap(p1, p3, false, false);
    *(u32x4*)(out + tok * DM + 32 * mb + 16 * (g & 1) + 8 * (g >> 1)) = u32x4{s0[0], s1[0], s0[1], s1[1]};
  }
  STAMPE(28);
}

extern "C" void kernel_launch(void* const* d_in, const int* in_sizes, int n_in, void* d_out, int out_size, void* d_ws, size_t ws_size,
                              hipStream_t stream) {
  (void)in_sizes; (void)n_in; (void)out_size; (void)ws_size;
  (void)hipFuncSetAttribute((const void*)fused_block, hipFuncAttributeMaxDynamicSharedMemorySize, LDS_BYTES);
  fused_block<<<256, 512, LDS_BYTES, stream>>>((const u16*)d_in[0], (const u16*)d_in[1], (const u16*)d_in[2], (const u16*)d_in[3],
                                               (const u16*)d_in[4], (const u16*)d_in[6], (const u16*)d_in[7], (const u16*)d_in[8],
                                               (const u16*)d_in[9], (const u16*)d_in[10], (char*)d_ws, (u16*)d_out);
}
```

```cpp
#include <hip/hip_runtime.h>
#include <hip/hip_bf16.h>
#include <stdint.h>

typedef unsigned short u16;
typedef unsigned int u32;
typedef unsigned long long u64;
typedef __bf16 bf16x8 __attribute__((ext_vector_type(8)));
typedef __bf16 bf16x2 __attribute__((ext_vector_type(2)));
typedef short s16x4 __attribute__((ext_vector_type(4)));
typedef float f32x16 __attribute__((ext_vector_type(16)));
typedef float f32x4 __attribute__((ext_vector_type(4)));
typedef float f32x2 __attribute__((ext_vector_type(2)));
typedef u32 u32x4 __attribute__((ext_vector_type(4)));
typedef u32 u32x2 __attribute__((ext_vector_type(2)));
#define LDSP __attribute__((address_space(3)))
#define GLBP __attribute__((address_space(1)))

constexpr int SEQ = 2048, DM = 512, FF = 1792;
constexpr float LOG2E = 1.4426950408889634f;

constexpr u32 WS_EPOCH = 0;
constexpr u32 WS_KN    = 1024;
constexpr u32 WS_PREP  = 2048;
constexpr u32 WS_MODG  = 4096;
constexpr u32 WS_MOD2  = 8192;
constexpr u32 WS_ST    = 16384;
constexpr u32 WS_KV    = WS_ST + 131072;
constexpr u32 WS_WOT   = WS_KV + 262144;
constexpr u32 WS_W1T   = WS_WOT + 524288;
constexpr u32 WS_WDT   = WS_W1T + 3584 * 1024;
constexpr u32 WS_OF    = WS_WDT + 1792 * 1024;
constexpr u32 WS_Y2F   = WS_OF + 2097152;
constexpr u32 WS_HF    = WS_Y2F + 2097152;
constexpr u32 WS_END   = WS_HF + 64 * 112 * 1024;

__device__ __forceinline__ float bf2f(u32 v) { return __uint_as_float(v << 16); }
__device__ __forceinline__ float lo2f(u32 w) { return __uint_as_float(w << 16); }
__device__ __forceinline__ float hi2f(u32 w) { return __uint_as_float(w & 0xffff0000u); }
__device__ __forceinline__ u32 pk2(float a, float b) {
  f32x2 v = {a, b};
  return __builtin_bit_cast(u32, __builtin_convertvector(v, bf16x2));
}
__device__ __forceinline__ float rbf(float a) { return lo2f(pk2(a, a)); }
__device__ __forceinline__ bf16x8 asbf(u32x4 v) { return __builtin_bit_cast(bf16x8, v); }
#define MFMA32(a, b, c) __builtin_amdgcn_mfma_f32_32x32x16_bf16(asbf(a), asbf(b), c, 0, 0, 0)
#define MFMA16(a, b, c) __builtin_amdgcn_mfma_f32_16x16x32_bf16(asbf(a), asbf(b), c, 0, 0, 0)
#define SBAR() __builtin_amdgcn_sched_barrier(0)
#define PIN(x) asm volatile("" : "+v"(x))
#define BAR() asm volatile("s_waitcnt lgkmcnt(0)\n\ts_barrier" ::: "memory")
#define WAITV(N) asm volatile("s_waitcnt vmcnt(" #N ")" ::: "memory")
#define WAITV_BAR(N) asm volatile("s_waitcnt vmcnt(" #N ") lgkmcnt(0)\n\ts_barrier" ::: "memory")
#define RLX_AGENT __ATOMIC_RELAXED, __HIP_MEMORY_SCOPE_AGENT
constexpr int SC1 = 16;
#define SPIN_LOG 16
constexpr int SPIN_CAP = 1 << SPIN_LOG;

template <int CTRL>
__device__ __forceinline__ float dpp(float x) {
  return __int_as_float(__builtin_amdgcn_update_dpp(0, __float_as_int(x), CTRL, 0xf, 0xf, true));
}
__device__ __forceinline__ float row_sum(float x) {
  x += dpp<0x128>(x); x += dpp<0x124>(x); x += dpp<0x122>(x); x += dpp<0x121>(x); return x;
}
__device__ __forceinline__ float row_max(float x) {
  x = fmaxf(x, dpp<0x128>(x)); x = fmaxf(x, dpp<0x124>(x)); x = fmaxf(x, dpp<0x122>(x)); x = fmaxf(x, dpp<0x121>(x)); return x;
}
__device__ __forceinline__ float swap32_add(float a, float b) {
  auto r = __builtin_amdgcn_permlane32_swap(__float_as_uint(a), __float_as_uint(b), false, false);
  return __uint_as_float(r[0]) + __uint_as_float(r[1]);
}
__device__ __forceinline__ float swap16_add(float a, float b) {
  auto r = __builtin_amdgcn_permlane16_swap(__float_as_uint(a), __float_as_uint(b), false, false);
  return __uint_as_float(r[0]) + __uint_as_float(r[1]);
}
__device__ __forceinline__ float swap32_sum(float x) { return swap32_add(x, x); }
__device__ __forceinline__ float swap16_sum(float x) { return swap16_add(x, x); }
__device__ __forceinline__ float swap32_max(float x) {
  auto r = __builtin_amdgcn_permlane32_swap(__float_as_uint(x), __float_as_uint(x), false, false);
  return fmaxf(__uint_as_float(r[0]), __uint_as_float(r[1]));
}
__device__ __forceinline__ float swap16_max(float x) {
  auto r = __builtin_amdgcn_permlane16_swap(__float_as_uint(x), __float_as_uint(x), false, false);
  return fmaxf(__uint_as_float(r[0]), __uint_as_float(r[1]));
}
__device__ __forceinline__ void reduce8(const float (&v)[8], float& r0, float& r1) {
  const float a0 = swap32_add(v[0], v[1]), a1 = swap32_add(v[2], v[3]), a2 = swap32_add(v[4], v[5]), a3 = swap32_add(v[6], v[7]);
  r0 = row_sum(swap16_add(a0, a1));
  r1 = row_sum(swap16_add(a2, a3));
}
__device__ __forceinline__ float rdlane(float x, int l) { return __int_as_float(__builtin_amdgcn_readlane(__float_as_int(x), l)); }
#define RED8_LANE(i) (16 * ((((i) & 1) << 1) | (((i) >> 1) & 1)))

extern __shared__ __attribute__((aligned(16))) char smem[];
__device__ __forceinline__ u32x4 lds_r128(u32 off) { return *(const LDSP u32x4*)((LDSP char*)smem + off); }
__device__ __forceinline__ void lds_w128(u32 off, u32x4 v) { *(LDSP u32x4*)((LDSP char*)smem + off) = v; }
__device__ __forceinline__ f32x4 lds_rf4(u32 off) { return *(const LDSP f32x4*)((LDSP char*)smem + off); }
__device__ __forceinline__ void lds_wf4(u32 off, f32x4 v) { *(LDSP f32x4*)((LDSP char*)smem + off) = v; }
__device__ __forceinline__ float lds_rf(u32 off) { return *(const LDSP float*)((LDSP char*)smem + off); }
__device__ __forceinline__ void lds_wf(u32 off, float v) { *(LDSP float*)((LDSP char*)smem + off) = v; }
__device__ __forceinline__ u32x2 lds_r64(u32 off) { return *(const LDSP u32x2*)((LDSP char*)smem + off); }
__device__ __forceinline__ void lds_w64(u32 off, u32x2 v) { *(LDSP u32x2*)((LDSP char*)smem + off) = v; }
__device__ __forceinline__ void lds_w32(u32 off, u32 v) { *(LDSP u32*)((LDSP char*)smem + off) = v; }
__device__ __forceinline__ u32 lds_r32(u32 off) { return *(const LDSP u32*)((LDSP char*)smem + off); }
__device__ __forceinline__ u32 lds_base() { return (u32)(uintptr_t)(LDSP char*)smem; }
typedef short v4i16_t __attribute__((ext_vector_type(4)));
__device__ __forceinline__ u32x2 lds_tr(u32 off) {
  return __builtin_bit_cast(u32x2, __builtin_amdgcn_ds_read_tr16_b64_v4i16((LDSP v4i16_t*)((LDSP char*)smem + off)));
}
__device__ __forceinline__ u32x4 lds_tr2(u32 off, u32 step) {
  const u32x2 a = lds_tr(off), c = lds_tr(off + step);
  return u32x4{a[0], a[1], c[0], c[1]};
}

#define GLDS_ASM(NAME, BODY)                                                                             \
  __device__ __forceinline__ void NAME(const void* g, u32 voff, u32 lds) {                               \
    u32 sv;                                                                                              \
    asm volatile("s_mov_b32 %0, m0\n\ts_mov_b32 m0, %3\n\ts_nop 0\n\t" BODY "s_mov_b32 m0, %0"           \
                 : "=&s"(sv) : "v"(voff), "s"(g), "s"(lds) : "memory");                                  \
  }
GLDS_ASM(glds1, "global_load_lds_dwordx4 %1, %2\n\t")
GLDS_ASM(glds1_sc1, "global_load_lds_dwordx4 %1, %2 sc1\n\t")
GLDS_ASM(glds4_sc1, "global_load_lds_dwordx4 %1, %2 sc1\n\tglobal_load_lds_dwordx4 %1, %2 offset:1024 sc1\n\t"
                    "global_load_lds_dwordx4 %1, %2 offset:2048 sc1\n\tglobal_load_lds_dwordx4 %1, %2 offset:3072 sc1\n\t")
#undef GLDS_ASM

__device__ __forceinline__ u32 ld4_sc1(const void* p) { return __hip_atomic_load((GLBP u32*)p, RLX_AGENT); }
__device__ __forceinline__ void st4_sc1(void* p, u32 v) { __hip_atomic_store((GLBP u32*)p, v, RLX_AGENT); }
__device__ __forceinline__ void st8_sc1(void* p, u32 lo, u32 hi) {
  __hip_atomic_store((GLBP u64*)p, ((u64)hi << 32) | lo, RLX_AGENT);
}
typedef __amdgpu_buffer_rsrc_t rsrc_t;
__device__ __forceinline__ u32x4 bld128(rsrc_t rs, u32 off) { return __builtin_amdgcn_raw_buffer_load_b128(rs, off, 0, SC1); }
__device__ __forceinline__ u32x2 bld64(rsrc_t rs, u32 off) { return __builtin_amdgcn_raw_buffer_load_b64(rs, off, 0, SC1); }
__device__ __forceinline__ void bst128(rsrc_t rs, u32 off, u32x4 v) { __builtin_amdgcn_raw_buffer_store_b128(v, rs, off, 0, SC1); }
__device__ __forceinline__ void bst64(rsrc_t rs, u32 off, u32x2 v) { __builtin_amdgcn_raw_buffer_store_b64(v, rs, off, 0, SC1); }
#define ORDER_BEHIND_POLL() __builtin_amdgcn_fence(__ATOMIC_ACQUIRE, "wavefront")

extern __shared__ __attribute__((aligned(16))) u32 smem32[];
__device__ __forceinline__ u32 lds_peek(u32 off) {
  return __hip_atomic_load((LDSP u32*)smem32 + off / 4, __ATOMIC_RELAXED, __HIP_MEMORY_SCOPE_WORKGROUP);
}
__device__ __forceinline__ void lds_post(u32 off, u32 v) {
  asm volatile("" ::: "memory");
  __hip_atomic_store((LDSP u32*)smem32 + off / 4, v, __ATOMIC_RELAXED, __HIP_MEMORY_SCOPE_WORKGROUP);
}
__device__ __forceinline__ u32 lds_arrive(u32 off, int lane) {
  asm volatile("" ::: "memory");
  u32 old = 0;
  if (lane == 0) old = __hip_atomic_fetch_add((LDSP u32*)smem32 + off / 4, 1u, __ATOMIC_RELAXED, __HIP_MEMORY_SCOPE_WORKGROUP);
  old = __builtin_amdgcn_readfirstlane(old);
  asm volatile("" ::: "memory");
  return old;
}
template <int SLEEP>
__device__ __forceinline__ void lds_wait(u32 off, u32 v) {
  _Pragma("unroll 1") for (int spin = 0; spin < SPIN_CAP; ++spin) {
    if ((u32)__builtin_amdgcn_readfirstlane(lds_peek(off)) >= v) break;
    __builtin_amdgcn_s_sleep(SLEEP);
  }
  asm volatile("" ::: "memory");
}
__device__ __forceinline__ int fresh_lane() {
  int l; asm volatile("v_mbcnt_lo_u32_b32 %0, -1, 0\n\tv_mbcnt_hi_u32_b32 %0, -1, %0" : "=v"(l)); return l;
}
constexpr u32 EMPTY = 0xffffffffu;
__device__ __forceinline__ u32 umax(u32 a, u32 b) { return a > b ? a : b; }
__device__ __forceinline__ u32 noempty(u32 v) { return v < EMPTY ? v : EMPTY - 1; }
__device__ __forceinline__ u32x4 noempty4(u32x4 v) { return u32x4{noempty(v[0]), noempty(v[1]), noempty(v[2]), noempty(v[3])}; }
template <int N>
__device__ __forceinline__ bool filled(const u32x4* f) {
  u32 m = 0, m2 = 0;
#pragma unroll
  for (int i = 0; i < N; ++i) { m = umax(umax(m, f[i][0]), f[i][1]); m2 = umax(umax(m2, f[i][2]), f[i][3]); }
  return __all(umax(m, m2) != EMPTY);
}
template <int N>
__device__ __forceinline__ void settle(rsrc_t rs, u32x4* f, u32 off) {
  if (__builtin_expect(!filled<N>(f), 0)) {
    _Pragma("unroll 1") for (int spin = 0; spin < SPIN_CAP; ++spin) {
#pragma unroll
      for (int i = 0; i < N; ++i) f[i] = bld128(rs, off + i * 1024);
      if (filled<N>(f)) break;
      __builtin_amdgcn_s_sleep(1);
    }
  }
}

#define STAMP(I)
#define STAMPE(I)
#define KV_DELAY 12
#define O_DELAY 10
#define Y_DELAY 10
#define Y_DELAY2 20
#define H_DELAY 1

constexpr u32 P_WQ = 0, P_H = 65536, P_WKV = 131072, P_M1 = 147456, P_PART = 149504;
constexpr u32 A_KV = 0, H_STG = 131072;
constexpr u32 T_WO = 65536, T_Y = 0, T_RED = 0, T_XCH = 131072, T_X2 = 147456,
              T_STX = 155648, T_ST = 156672, T_US = 157696;
constexpr u32 L_ONES = 155648, L_SYNC = 159616, L_MODI = 159744, LDS_BYTES = 163840;
constexpr u32 S_PROG = L_SYNC, S_CONS = L_SYNC + 16, S_PARR = L_SYNC + 20, S_PREP = L_SYNC + 24, S_STARR = L_SYNC + 28,
              S_YV = L_SYNC + 32, S_STOK = L_SYNC + 36, S_KM2 = L_SYNC + 40, S_T1D = L_SYNC + 44, S_XCH = L_SYNC + 48;
constexpr u32 M_GA = L_MODI, M_SB = L_MODI + 1024, M_CB = L_MODI + 2048, M_GB = L_MODI + 3072, M_SILU = L_MODI;
constexpr float QSCALE = 0.25f * LOG2E;
constexpr float BOUND2 = 64.f * 64.f;
#define EXP2(x) __builtin_amdgcn_exp2f(x)

__device__ __forceinline__ u32x4 ald128(const void* g, u32 voff) {
  u32x4 v; asm volatile("global_load_dwordx4 %0, %1, %2" : "=v"(v) : "v"(voff), "s"(g) : "memory"); return v;
}
__device__ __forceinline__ u32 ald16(const void* g, u32 voff) {
  u32 v; asm volatile("global_load_ushort %0, %1, %2" : "=v"(v) : "v"(voff), "s"(g) : "memory"); return v;
}
__device__ __forceinline__ u32 ald32_sc1(const void* g, u32 voff) {
  u32 v; asm volatile("global_load_dword %0, %1, %2 sc1" : "=v"(v) : "v"(voff), "s"(g) : "memory"); return v;
}
__device__ __forceinline__ void glds1_v(const void* p, u32 lds) {
  u32 sv;
  asm volatile("s_mov_b32 %0, m0\n\ts_mov_b32 m0, %2\n\ts_nop 0\n\tglobal_load_lds_dwordx4 %1, off\n\ts_mov_b32 m0, %0"
               : "=&s"(sv) : "v"(p), "s"(lds) : "memory");
}

#define HALF(Sc, Sn, Kn, Qn, Oc, V0, V1, CIN)                                                          \
  do {                                                                                                 \
    u32x4 p0_, p1_;                                                                                    \
    Sc[0] = EXP2(Sc[0]); Sc[1] = EXP2(Sc[1]); Sc[2] = EXP2(Sc[2]); Sc[3] = EXP2(Sc[3]);                \
    p0_[0] = pk2(Sc[0], Sc[1]); p0_[1] = pk2(Sc[2], Sc[3]); PIN(p0_[0]); PIN(p0_[1]); SBAR();          \
    Sn = MFMA32(Kn, Qn, CIN); SBAR();                                                                  \
    Sc[4] = EXP2(Sc[4]); Sc[5] = EXP2(Sc[5]); Sc[6] = EXP2(Sc[6]); Sc[7] = EXP2(Sc[7]);                \
    p0_[2] = pk2(Sc[4], Sc[5]); p0_[3] = pk2(Sc[6], Sc[7]); PIN(p0_); SBAR();                          \
    Oc = MFMA32(V0, p0_, Oc); SBAR();                                                                  \
    Sc[8] = EXP2(Sc[8]); Sc[9] = EXP2(Sc[9]); Sc[10] = EXP2(Sc[10]); Sc[11] = EXP2(Sc[11]);            \
    p1_[0] = pk2(Sc[8], Sc[9]); p1_[1] = pk2(Sc[10], Sc[11]); PIN(p1_[0]); PIN(p1_[1]); SBAR();        \
    Sc[12] = EXP2(Sc[12]); Sc[13] = EXP2(Sc[13]); Sc[14] = EXP2(Sc[14]); Sc[15] = EXP2(Sc[15]);        \
    p1_[2] = pk2(Sc[12], Sc[13]); p1_[3] = pk2(Sc[14], Sc[15]); PIN(p1_); SBAR();                      \
    Oc = MFMA32(V1, p1_, Oc); SBAR();                                                                  \
  } while (0)
#define TWO_TILES()                                                                                    \
  do {                                                                                                 \
    const u32 ka = A_KV + t * 2048 + vo;                                                               \
    u32x4 Kn = lds_r128(ka + 2048);                                                                    \
    u32x4 W0 = lds_r128(vadr + 2048), W1 = lds_r128(vadr + 2560);                                      \
    SBAR();                                                                                            \
    HALF(Sa, Sb, Kc, Qb, Oa, V0, V1, zero);                                                            \
    HALF(Sb, Sa, Kn, Qa, Ob, V0, V1, zero);                                                            \
    vadr += 2 * vstep;                                                                                 \
    Kc = lds_r128(ka + 4096);                                                                          \
    V0 = lds_r128(vadr); V1 = lds_r128(vadr + 512);                                                    \
    SBAR();                                                                                            \
    HALF(Sa, Sb, Kn, Qb, Oa, W0, W1, zero);                                                            \
    HALF(Sb, Sa, Kc, Qa, Ob, W0, W1, zero);                                                            \
  } while (0)
__device__ __forceinline__ void wait_kv(u32 n) {
  _Pragma("unroll 1") for (int spin = 0; spin < SPIN_CAP; ++spin) {
    asm volatile("" ::: "memory");
    const u32x2 p = lds_r64(S_PROG);
    if ((u32)__builtin_amdgcn_readfirstlane(p[0] < p[1] ? p[0] : p[1]) >= n) break;
    __builtin_amdgcn_s_sleep(1);
  }
  asm volatile("" ::: "memory");
}
#define CHAIN32(ACC, ADDR, STRIDE, DEPTH, EXTRA)                                                       \
  {                                                                                                    \
    u32x4 ring_[DEPTH];                                                                                \
    _Pragma("unroll") for (int i = 0; i < DEPTH; ++i) ring_[i] = lds_r128((ADDR) + i * (STRIDE));      \
    SBAR();                                                                                            \
    _Pragma("unroll") for (int i = 0; i < 32; ++i) {                                                   \
      ACC = MFMA32(w[i], ring_[i % DEPTH], ACC);                                                       \
      if (i + DEPTH < 32) ring_[i % DEPTH] = lds_r128((ADDR) + (i + DEPTH) * (STRIDE));                \
      EXTRA;                                                                                           \
      SBAR();                                                                                          \
    }                                                                                                  \
  }
#define COLUMN_SUMS()                                                                                  \
  {                                                                                                    \
    f32x16 us = {};                                                                                    \
    const int ln_ = fresh_lane();                                                                      \
    const u32 ma = ((ln_ & 1) ? M_SB : M_CB) + (ln_ >> 5) * 16;                                        \
    CHAIN32(us, ma, 32, 8, );                                                                          \
    if ((ln_ & 31) < 2) {                                                                              \
      const float sc_ = (ln_ & 1) ? -LOG2E : 1.f;                                                      \
      const u32 ua = T_US + (at - 7 * cs) * 256 + (ln_ & 1) * 128 + (ln_ >> 5) * 64;                   \
      lds_wf4(ua, f32x4{us[0] * sc_, us[1] * sc_, us[2] * sc_, us[3] * sc_});                          \
      lds_wf4(ua + 16, f32x4{us[4] * sc_, us[5] * sc_, us[6] * sc_, us[7] * sc_});                     \
      lds_wf4(ua + 32, f32x4{us[8], us[9], us[10], us[11]});                                           \
      lds_wf4(ua + 48, f32x4{us[12], us[13], us[14], us[15]});                                         \
    }                                                                                                  \
  }

__global__ __launch_bounds__(512, 2) void fused_block(const u16* __restrict__ x, const u16* __restrict__ wq, const u16* __restrict__ wk,
                                                      const u16* __restrict__ wv, const u16* __restrict__ wo, const u16* __restrict__ wg,
                                                      const u16* __restrict__ wu, const u16* __restrict__ wd, const u16* __restrict__ cvec,
                                                      const u16* __restrict__ wmod, char* __restrict__ ws, u16* __restrict__ out) {
  const int tid = threadIdx.x, lane = tid & 63, b = blockIdx.x;
  const int wave = __builtin_amdgcn_readfirstlane(tid >> 6);
  const int r = lane & 31, hh = lane >> 5, g = lane >> 4, li = lane & 15;
  const int xcd = b & 7, idx = b >> 3;
  const int tg = xcd * 4 + (idx >> 3), hg = idx & 7;
  const int ng = hg, G = tg >> 1, cs = idx & 15;
  const u32 lb = lds_base(), vo = lane * 16;
  const rsrc_t rs = __builtin_amdgcn_make_buffer_rsrc((void*)ws, 0, WS_END, 0x00020000);
  const int tq_ = li >> 2, tp_ = lane & 3;
  STAMP(0);

  const int colA = 64 * (xcd + 8 * ((idx >> 3) & 1)) + 8 * (idx & 7);
  u32 ev = ald32_sc1(ws + WS_EPOCH, 0);
  u32 cv = ald16(cvec, tid * 2);
  u32x4 wA = ald128(wmod, idx < 16 ? tid * 6144 + colA * 2 : 0);
  const int kq = hg >> 1;
  auto row_of = [&](int i) { return i < 2 ? 16 * kq + 2 * wave + i : (16 * kq + 14 + 6 * wave + i) & 63; };
  u32x4 xr[8];
#pragma unroll
  for (int i = 0; i < 8; ++i) xr[i] = ald128(x + (64 * tg + row_of(i)) * DM, vo);
  lds_w32(L_ONES + tid * 4, 0x3f803f80u);
  if (tid < 256) lds_w32(L_ONES + 2048 + tid * 4, 0x3f803f80u);
  if (tid < 16) lds_w32(L_SYNC + tid * 4, 0u);
  asm volatile("s_waitcnt vmcnt(8)" : "+v"(ev), "+v"(cv), "+v"(wA)::"memory");
  const u32 tag = __builtin_amdgcn_readfirstlane(ev) + 1;
  {
    const float f = bf2f(cv), sl = rbf(f / (1.f + __expf(-f)));
    *(LDSP u16*)((LDSP char*)smem + M_SILU + tid * 2) = (u16)(__float_as_uint(sl) >> 16);
    if (idx < 16) {
      float a[8], r0, r1;
      const int sub = ((g & 1) << 1) | (g >> 1);
#pragma unroll
      for (int i = 0; i < 4; ++i) { a[2 * i] = sl * lo2f(wA[i]); a[2 * i + 1] = sl * hi2f(wA[i]); }
      reduce8(a, r0, r1);
      if (li == 0) { lds_wf(P_PART + (wave * 8 + sub) * 4, r0); lds_wf(P_PART + (wave * 8 + 4 + sub) * 4, r1); }
      BAR();
      if (wave == 0) {
        const int jl = lane & 7;
        float s = 0;
#pragma unroll
        for (int w = 0; w < 8; ++w) s += lds_rf(P_PART + (w * 8 + jl) * 4);
        s = rbf(s);
        const int col = colA + jl;
        if (col >= 512) s = rbf(1.f + s);
        const float sn = dpp<0xB1>(s);
        if (lane < 8 && !(lane & 1)) st8_sc1(ws + WS_MODG + (col >> 1) * 8, tag, pk2(s, sn));
      }
    }
  }
  STAMP(1);
  float xh[8][8];
  asm volatile("s_waitcnt vmcnt(0)"
               : "+v"(xr[0]), "+v"(xr[1]), "+v"(xr[2]), "+v"(xr[3]), "+v"(xr[4]), "+v"(xr[5]), "+v"(xr[6]), "+v"(xr[7])::"memory");
  if (wave < 4) {
    const u32 vq = (lane >> 3) * 1024 + hg * 128 + (((lane & 7) ^ (((lane >> 4) & 1) << 2)) * 16);
#pragma unroll
    for (int i = 0; i < 16; ++i) glds1(wq + (128 * wave + 8 * i) * DM, vq, lb + P_WQ + (128 * wave + 8 * i) * 128);
  } else if (wave == 7) {
    const u16* wkv = (hg & 1) ? wv : wk;
#pragma unroll
    for (int i = 0; i < 16; ++i) glds1(wkv + i * 512, vo, lb + P_WKV + i * 1024);
  }
  STAMP(2);
  {
    float s[8], r0, r1;
#pragma unroll
    for (int i = 0; i < 8; ++i) {
#pragma unroll
      for (int j = 0; j < 4; ++j) { xh[i][2 * j] = lo2f(xr[i][j]); xh[i][2 * j + 1] = hi2f(xr[i][j]); }
      s[i] = ((xh[i][0] + xh[i][1]) + (xh[i][2] + xh[i][3])) + ((xh[i][4] + xh[i][5]) + (xh[i][6] + xh[i][7]));
    }
    reduce8(s, r0, r1);
#pragma unroll
    for (int i = 0; i < 8; ++i) {
      const float m = rdlane(i < 4 ? r0 : r1, RED8_LANE(i)) * (1.f / DM);
      float q = 0;
#pragma unroll
      for (int j = 0; j < 8; ++j) { xh[i][j] -= m; q += xh[i][j] * xh[i][j]; }
      s[i] = q;
    }
    reduce8(s, r0, r1);
    r0 = rsqrtf(r0 * (1.f / DM) + 1e-5f); r1 = rsqrtf(r1 * (1.f / DM) + 1e-5f);
#pragma unroll
    for (int i = 0; i < 8; ++i) {
      const float rstd = rdlane(i < 4 ? r0 : r1, RED8_LANE(i));
#pragma unroll
      for (int j = 0; j < 8; ++j) xh[i][j] *= rstd;
    }
  }
  STAMP(3);
  u32x4 wf[16];
  if (wave == 6) {
    _Pragma("unroll 1") for (int spin = 0; spin < SPIN_CAP; ++spin) {
      u32x4 q[4];
#pragma unroll
      for (int i = 0; i < 4; ++i) q[i] = bld128(rs, WS_MODG + i * 1024 + vo);
      bool ok = true;
#pragma unroll
      for (int i = 0; i < 4; ++i) { ok &= (q[i][0] == tag) & (q[i][2] == tag); lds_w64(P_M1 + i * 512 + lane * 8, u32x2{q[i][1], q[i][3]}); }
      if (__all(ok)) break;
      __builtin_amdgcn_s_sleep(1);
    }
  } else if (wave == 7) {
    WAITV(0);
    const u32 wa = P_WKV + (8 * g + tq_) * 32 + 8 * tp_;
#pragma unroll
    for (int t = 0; t < 16; ++t) wf[t] = lds_tr2(wa + t * 1024, 128);
  }
  BAR();
  STAMP(4);
  {
    const u32x4 sa = lds_r128(P_M1 + vo), ca = lds_r128(P_M1 + 1024 + vo);
    float sf[8], cf[8];
#pragma unroll
    for (int j = 0; j < 4; ++j) { sf[2 * j] = lo2f(sa[j]); sf[2 * j + 1] = hi2f(sa[j]); cf[2 * j] = lo2f(ca[j]); cf[2 * j + 1] = hi2f(ca[j]); }
    auto put_row = [&](int i) {
      const int rho = row_of(i), rr = rho & 31;
      u32x4 hv;
#pragma unroll
      for (int j = 0; j < 4; ++j) hv[j] = pk2(xh[i][2 * j] * cf[2 * j] + sf[2 * j], xh[i][2 * j + 1] * cf[2 * j + 1] + sf[2 * j + 1]);
      lds_w128(P_H + (rho >> 5) * 32768 + (lane >> 1) * 1024 + (lane & 1) * 512 + ((rr ^ li) * 16), hv);
    };
    put_row(0); put_row(1);
    BAR();
    if (wave == 7) {
      __builtin_amdgcn_s_setprio(3);
      const int q = kq, m = hg & 1;
      const int rr = (q & 1) * 16 + li;
      f32x4 d0 = {0, 0, 0, 0}, d1 = {0, 0, 0, 0};
      const u32 ha = P_H + (q >> 1) * 32768 + (g >> 1) * 1024 + (g & 1) * 512;
      u32x4 hf[16];
#pragma unroll
      for (int t = 0; t < 16; ++t) hf[t] = lds_r128(ha + t * 2048 + ((rr ^ ((4 * t + g) & 15)) * 16));
      SBAR();
      if (m == 0) {
#pragma unroll
        for (int t = 0; t < 16; t += 2) { d0 = MFMA16(wf[t], hf[t], d0); d1 = MFMA16(wf[t + 1], hf[t + 1], d1); }
      } else {
#pragma unroll
        for (int t = 0; t < 16; t += 2) { d0 = MFMA16(hf[t], wf[t], d0); d1 = MFMA16(hf[t + 1], wf[t + 1], d1); }
      }
      d0 += d1;
      const u32x2 pkd = {pk2(d0[0], d0[1]), pk2(d0[2], d0[3])};
      const u32 dst = WS_KV + (2 * tg + (q >> 1)) * 4096 + (g >> 1) * 1024;
      if (m == 0) {
        bst128(rs, dst + ((g & 1) * 32 + rr) * 16, u32x4{tag, pkd[0], tag, pkd[1]});
        const float k0 = lo2f(pkd[0]), k1 = hi2f(pkd[0]), k2 = lo2f(pkd[1]), k3 = hi2f(pkd[1]);
        const float kn2 = row_max(swap32_sum(swap16_sum(k0 * k0 + k1 * k1 + k2 * k2 + k3 * k3)));
        if (lane == 0) st8_sc1(ws + WS_KN + (tg * 4 + q) * 8, tag, __float_as_uint(kn2));
      } else {
        bst128(rs, dst + 2048 + ((q & 1) * 32 + (g & 1) * 16 + li) * 16, u32x4{tag, pkd[0], tag, pkd[1]});
      }
      __builtin_amdgcn_s_setprio(0);
      STAMP(26);
    }
#pragma unroll
    for (int i = 2; i < 8; ++i) put_row(i);
  }
  if (wave < 4) WAITV(0);
  BAR();
  STAMP(5);

  u32x4 w[32];
  const int at = 7 * cs + (wave < 4 ? 4 + wave : wave - 4);
  if (wave < 4) {
    const int th = wave & 1, hp = wave >> 1;
    u32x4 Qa, Qb; float qn2;
    {
      f32x16 qa = {};
      const u32 a0 = P_WQ + (8 * (g >> 1) + tq_) * 128 + (((4 * hp + 2 * (g & 1) + (tp_ >> 1)) ^ ((tq_ >> 1) << 2)) * 16) + 8 * (tp_ & 1);
      u32x4 ra[4], rb[4];
      auto rdq = [&](int ks) {
        ra[ks & 3] = lds_tr2(a0 + ks * 2048, 512);
        rb[ks & 3] = lds_r128(P_H + th * 32768 + ks * 1024 + hh * 512 + ((r ^ ((2 * ks + hh) & 15)) * 16));
      };
#pragma unroll
      for (int ks = 0; ks < 4; ++ks) rdq(ks);
      SBAR();
#pragma unroll
      for (int ks = 0; ks < 32; ++ks) {
        qa = MFMA32(ra[ks & 3], rb[ks & 3], qa);
        if (ks + 4 < 32) rdq(ks + 4);
        SBAR();
      }
      lds_arrive(S_CONS, lane);
#pragma unroll
      for (int j = 0; j < 4; ++j) {
        Qa[j] = pk2(qa[2 * j] * QSCALE, qa[2 * j + 1] * QSCALE); Qb[j] = pk2(qa[8 + 2 * j] * QSCALE, qa[9 + 2 * j] * QSCALE);
      }
      float na = 0, nb = 0;
#pragma unroll
      for (int j = 0; j < 4; ++j) {
        na += lo2f(Qa[j]) * lo2f(Qa[j]) + hi2f(Qa[j]) * hi2f(Qa[j]);
        nb += lo2f(Qb[j]) * lo2f(Qb[j]) + hi2f(Qb[j]) * hi2f(Qb[j]);
      }
      qn2 = fmaxf(swap32_sum(na), swap32_sum(nb));
    }
    STAMP(6);
    f32x16 Oa = {}, Ob = {};
    const u32 vadr0 = (r < 16) ? (A_KV + 1024 + hh * 256 + r * 16) : (L_ONES + ((r >= 20 && r < 28) ? 64 : 0));
    const u32 vstep = (r < 16) ? 2048u : 0u;
    {
      f32x16 Sa, Sb;
      const f32x16 zero = {};
      u32 vadr = vadr0;
      wait_kv(1);
      __builtin_amdgcn_s_setprio(1);
      STAMP(7);
      u32x4 Kc = lds_r128(A_KV + vo);
      Sa = MFMA32(Kc, Qa, zero);
      u32x4 V0 = lds_r128(vadr), V1 = lds_r128(vadr + 512);
      int t = 0;
#pragma unroll 1
      for (int seg = 0; seg < 5; ++seg) {
        const int tend = seg == 0 ? 6 : seg == 1 ? 14 : seg == 2 ? 30 : seg == 3 ? 38 : 56;
#pragma unroll 1
        for (; t < tend; t += 2) TWO_TILES();
        if (seg < 4) wait_kv(seg == 0 ? 2 : seg == 1 ? 4 : seg == 2 ? 5 : 8);
      }
      STAMP(8);
      lds_wait<1>(S_PREP, 1);
#pragma unroll
      for (int u = 0; u < 4; ++u) {
        if (wave < 3) {
#pragma unroll
          for (int i = 8 * u; i < 8 * u + 8; ++i) w[i] = bld128(rs, WS_W1T + (at * 32 + i) * 1024 + vo);
        }
        SBAR();
        TWO_TILES();
        t += 2;
      }
    }
    __builtin_amdgcn_s_setprio(0);
    STAMP(9);
    if (__any(!(qn2 * lds_rf(S_KM2) <= BOUND2))) {
#pragma unroll 1
      for (int x2 = 0; x2 < 2; ++x2) {
        const u32x4 Q = x2 ? Qb : Qa;
        float m = -3.0e38f;
        const f32x16 zero = {};
#pragma unroll 1
        for (int t = 0; t < 64; ++t) {
          const f32x16 s = MFMA32(lds_r128(A_KV + t * 2048 + vo), Q, zero);
#pragma unroll
          for (int j = 0; j < 16; ++j) m = fmaxf(m, s[j]);
        }
        m = swap32_max(m);
        f32x16 cm;
#pragma unroll
        for (int j = 0; j < 16; ++j) cm[j] = -m;
        f32x16 O = {};
        u32 va = vadr0;
#pragma unroll 1
        for (int t = 0; t < 64; ++t) {
          f32x16 s = MFMA32(lds_r128(A_KV + t * 2048 + vo), Q, cm);
          u32x4 p0, p1;
#pragma unroll
          for (int j = 0; j < 4; ++j) { p0[j] = pk2(EXP2(s[2 * j]), EXP2(s[2 * j + 1])); p1[j] = pk2(EXP2(s[8 + 2 * j]), EXP2(s[9 + 2 * j])); }
          O = MFMA32(lds_r128(va), p0, O);
          O = MFMA32(lds_r128(va + 512), p1, O);
          va += vstep;
        }
        if (x2) Ob = O; else Oa = O;
      }
    }
    {
      const float ia = __builtin_amdgcn_rcpf(Oa[8]), ib = __builtin_amdgcn_rcpf(Ob[8]);
      const u32x4 oa = {pk2(Oa[0] * ia, Oa[1] * ia), pk2(Oa[2] * ia, Oa[3] * ia), pk2(Oa[4] * ia, Oa[5] * ia), pk2(Oa[6] * ia, Oa[7] * ia)};
      const u32x4 ob = {pk2(Ob[0] * ib, Ob[1] * ib), pk2(Ob[2] * ib, Ob[3] * ib), pk2(Ob[4] * ib, Ob[5] * ib), pk2(Ob[6] * ib, Ob[7] * ib)};
      const u32 o = WS_OF + ((2 * tg + th) * 32 + 4 * hg + 2 * hp) * 1024 + vo;
      bst128(rs, o, noempty4(oa));
      bst128(rs, o + 1024, noempty4(ob));
    }
    STAMP(10);
    {
      u32x4 f[16];
      const u32 src = WS_OF + ((2 * tg + th) * 32 + 16 * hp) * 1024 + vo;
      __builtin_amdgcn_s_sleep(O_DELAY);
#pragma unroll
      for (int i = 0; i < 16; ++i) f[i] = bld128(rs, src + i * 1024);
      asm volatile("s_barrier" ::: "memory");
      asm volatile("s_barrier" ::: "memory");
      STAMP(11);
      const u32 wa = T_WO + 16 * hp * 1024 + vo;
      const u32 wk_ = wa + hp * 32768, ws_ = wa + (1 - hp) * 32768;
      u32x4 ring[4];
      auto rdw = [&](int s) { ring[s & 3] = lds_r128(((s & 1) ? ws_ : wk_) + (s >> 1) * 1024); };
#pragma unroll
      for (int s = 0; s < 4; ++s) rdw(s);
      f32x16 ak = {}, as = {};
      settle<8>(rs, f, src);
      STAMP(12);
#pragma unroll
      for (int s = 0; s < 32; ++s) {
        if (s == 16) settle<8>(rs, f + 8, src + 8192);
        if (s & 1) as = MFMA32(ring[s & 3], f[s >> 1], as); else ak = MFMA32(ring[s & 3], f[s >> 1], ak);
        if (s + 4 < 32) rdw(s + 4);
        SBAR();
      }
      STAMP(13);
#pragma unroll
      for (int q = 0; q < 4; ++q) lds_wf4(T_XCH + wave * 4096 + q * 1024 + vo, f32x4{as[4 * q], as[4 * q + 1], as[4 * q + 2], as[4 * q + 3]});
      lds_post(S_XCH + wave * 4, 1);
      const int ln = fresh_lane(), r5 = ln & 31, h5 = ln >> 5;
      const u32 xa_ = T_X2 + (32 * th + r5) * 128 + h5 * 8;
      u32x2 xres[4], ga[4];
      u32x4 cb[2];
#pragma unroll
      for (int q = 0; q < 4; ++q) {
        xres[q] = lds_r64(xa_ + (((4 * hp + q) ^ (r5 & 7)) * 16));
        ga[q] = lds_r64(M_GA + (64 * ng + 32 * hp + 8 * q + 4 * h5) * 2);
      }
#pragma unroll
      for (int s = 0; s < 2; ++s) cb[s] = lds_r128(M_CB + (4 * ng + 2 * hp + s) * 32 + h5 * 16);
      lds_wait<0>(S_XCH + (wave ^ 2) * 4, 1);
      u32 xp[8];
      float sm = 0, sq = 0;
#pragma unroll
      for (int q = 0; q < 4; ++q) {
        const f32x4 o4 = lds_rf4(T_XCH + (wave ^ 2) * 4096 + q * 1024 + ln * 16);
        xp[2 * q] = pk2(lo2f(xres[q][0]) + lo2f(ga[q][0]) * (ak[4 * q] + o4[0]), hi2f(xres[q][0]) + hi2f(ga[q][0]) * (ak[4 * q + 1] + o4[1]));
        xp[2 * q + 1] = pk2(lo2f(xres[q][1]) + lo2f(ga[q][1]) * (ak[4 * q + 2] + o4[2]), hi2f(xres[q][1]) + hi2f(ga[q][1]) * (ak[4 * q + 3] + o4[3]));
      }
#pragma unroll
      for (int s = 0; s < 2; ++s) {
        u32x4 y;
#pragma unroll
        for (int d = 0; d < 4; ++d) y[d] = pk2(lo2f(xp[4 * s + d]) * lo2f(cb[s][d]), hi2f(xp[4 * s + d]) * hi2f(cb[s][d]));
        bst128(rs, WS_Y2F + ((2 * tg + th) * 32 + 4 * ng + 2 * hp + s) * 1024 + ln * 16, noempty4(y));
      }
      SBAR();
      STAMP(14);
#pragma unroll
      for (int q = 0; q < 4; ++q) {
        lds_w64(xa_ + (((4 * hp + q) ^ (r5 & 7)) * 16), u32x2{xp[2 * q], xp[2 * q + 1]});
        const float x0 = lo2f(xp[2 * q]), x1 = hi2f(xp[2 * q]), x2_ = lo2f(xp[2 * q + 1]), x3 = hi2f(xp[2 * q + 1]);
        sm += (x0 + x1) + (x2_ + x3);
        sq += (x0 * x0 + x1 * x1) + (x2_ * x2_ + x3 * x3);
      }
      lds_wf(T_STX + hp * 512 + (32 * th + r5) * 8 + h5 * 4, swap32_add(sm, sq));
      if (lds_arrive(S_STARR, ln) == 3) {
        const u32x2 s0 = lds_r64(T_STX + ln * 8), s1 = lds_r64(T_STX + 512 + ln * 8);
        bst64(rs, WS_ST + (tg * 8 + ng) * 512 + ln * 8,
              u32x2{noempty(__float_as_uint(__uint_as_float(s0[0]) + __uint_as_float(s1[0]))),
                    noempty(__float_as_uint(__uint_as_float(s0[1]) + __uint_as_float(s1[1])))});
      }
    }
    if (wave < 3) COLUMN_SUMS();
    STAMP(15);
  } else {
    const int j = wave - 4;
    if (wave < 6) {
      u32x4 qa_[16], qb_[16];
      auto request = [&](u32x4 (&q)[16], int n) {
        const u32 src = WS_KV + (8 * n + 4 * j) * 4096 + vo;
#pragma unroll
        for (int i = 0; i < 16; ++i) q[i] = bld128(rs, src + i * 1024);
      };
      auto tagged = [&](const u32x4 (&q)[16]) {
        u32 bad = 0;
#pragma unroll
        for (int i = 0; i < 16; ++i) bad |= (q[i][0] ^ tag) | (q[i][2] ^ tag);
        return (bool)__all(bad == 0);
      };
      auto complete = [&](u32x4 (&q)[16], int n) {
        if (__builtin_expect(!tagged(q), 0)) {
          _Pragma("unroll 1") for (int spin = 0; spin < SPIN_CAP; ++spin) {
            request(q, n);
            if (tagged(q)) break;
            __builtin_amdgcn_s_sleep(1);
          }
        }
      };
      auto deliver = [&](const u32x4 (&q)[16], int n) {
#pragma unroll
        for (int i = 0; i < 8; ++i)
          lds_w128(A_KV + (8 * n + 4 * j) * 2048 + i * 1024 + vo, u32x4{q[2 * i][1], q[2 * i][3], q[2 * i + 1][1], q[2 * i + 1][3]});
        lds_post(S_PROG + j * 4, n + 1);
      };
      __builtin_amdgcn_s_sleep(KV_DELAY);
      request(qa_, 0);
      complete(qa_, 0);
      STAMP(7);
      request(qb_, 1);
      lds_wait<1>(S_CONS, 4);
      deliver(qa_, 0);
#pragma unroll 1
      for (int n = 1; n < 7; n += 2) {
        request(qa_, n + 1);
        complete(qb_, n);
        deliver(qb_, n);
        request(qb_, n + 2);
        complete(qa_, n + 1);
        deliver(qa_, n + 1);
      }
      complete(qb_, 7);
      deliver(qb_, 7);
      STAMP(8);
      lds_wait<8>(S_PREP, 1);
    } else {
      const int jj = wave - 6;
      lds_wait<4>(S_PROG, 1);
      STAMP(7);
      const int col = 64 * (xcd + 8 * (idx >> 3)) + 8 * (idx & 7);
      u32x4 wm[8];
      if (wave == 6) {
#pragma unroll
        for (int i = 0; i < 8; ++i) wm[i] = *(const u32x4*)((const char*)wmod + (64 * i + lane) * 6144 + (1024 + col) * 2);
      }
#pragma unroll
      for (int i3 = 0; i3 < 3; ++i3) {
        const int u = (3 * jj + i3) * 256 + xcd * 32 + idx;
        if (u < 1472) {
          const int R = lane >> 3, cp = lane & 7;
          const char* src; u32 ld;
          if (u < 128)       { src = (const char*)wo + (32 * (u >> 3) + R) * 1024 + (u & 7) * 128 + cp * 16; ld = 1024; }
          else if (u < 1024) { const int e = u - 128, kb = e / 56, ab = e - kb * 56;
                               src = (const char*)(cp < 4 ? wg : wu) + (32 * kb + R) * (FF * 2) + ab * 64 + (cp & 3) * 16; ld = FF * 2; }
          else               { const int e = u - 1024; src = (const char*)wd + (32 * (e >> 3) + R) * 1024 + (e & 7) * 128 + cp * 16; ld = 1024; }
#pragma unroll
          for (int i = 0; i < 4; ++i) glds1_v(src + i * 8 * ld, lb + H_STG + (3 * jj + i3) * 4096 + i * 1024);
        }
      }
      const u32x4 e4 = {EMPTY, EMPTY, EMPTY, EMPTY};
      {
        const u32 o = (jj ? WS_Y2F : WS_OF) + (2 * tg * 32 + 4 * hg) * 1024 + vo;
#pragma unroll
        for (int i = 0; i < 8; ++i) bst128(rs, o + (i >> 2) * 32768 + (i & 3) * 1024, e4);
#pragma unroll
        for (int i = 0; i < 14; ++i) bst128(rs, WS_HF + ((4 * G + 2 * jj + i / 7) * 112 + 7 * cs + i % 7) * 1024 + vo, e4);
      }
      WAITV(22);
      if (wave == 7 && lane < 32) bst128(rs, WS_ST + (tg * 8 + ng) * 512 + vo, e4);
#pragma unroll
      for (int i3 = 0; i3 < 3; ++i3) {
        const int u = (3 * jj + i3) * 256 + xcd * 32 + idx;
        const u32 sb = H_STG + (3 * jj + i3) * 4096 + tq_ * 128 + 8 * tp_;
        if (u >= 128 && u < 1024) {
          const int e = u - 128, kb = e / 56, ab = e - kb * 56;
#pragma unroll
          for (int f = 0; f < 4; ++f)
            bst128(rs, WS_W1T + ((2 * ab + (f >> 1)) * 32 + 2 * kb + (f & 1)) * 1024 + vo,
                   lds_tr2(sb + (16 * (f & 1) + 4 * (g >> 1)) * 128 + (g & 1) * 64 + 32 * (f >> 1), 1024));
        } else if (u < 1472) {
          const int e = u < 128 ? u : u - 1024;
          const u32 dst = u < 128 ? WS_WOT : WS_WDT, nks = u < 128 ? 32 : 112;
#pragma unroll
          for (int f = 0; f < 4; ++f)
            bst128(rs, dst + ((2 * (e & 7) + (f >> 1)) * nks + 2 * (e >> 3) + (f & 1)) * 1024 + vo,
                   lds_tr2(sb + (16 * (f & 1) + 4 * (g >> 1)) * 128 + 64 * (f >> 1) + 32 * (g & 1), 1024));
        }
      }
      if (wave == 6) {
        float a[8] = {0, 0, 0, 0, 0, 0, 0, 0}, r0, r1;
#pragma unroll
        for (int i = 0; i < 8; ++i) {
          const float sl = bf2f(*(const LDSP u16*)((LDSP char*)smem + M_SILU + (64 * i + lane) * 2));
#pragma unroll
          for (int c = 0; c < 4; ++c) { a[2 * c] += sl * lo2f(wm[i][c]); a[2 * c + 1] += sl * hi2f(wm[i][c]); }
        }
        reduce8(a, r0, r1);
        float mv[8];
#pragma unroll
        for (int i = 0; i < 8; ++i) {
          mv[i] = rbf(rdlane(i < 4 ? r0 : r1, RED8_LANE(i)));
          if ((col >> 9) == 2) mv[i] = rbf(1.f + mv[i]);
        }
        if (lane == 0) bst128(rs, WS_MOD2 + col * 2, u32x4{pk2(mv[0], mv[1]), pk2(mv[2], mv[3]), pk2(mv[4], mv[5]), pk2(mv[6], mv[7])});
      }
      WAITV(0);
      STAMP(8);
      if (lds_arrive(S_PARR, lane) == 1 && lane == 0) st4_sc1(ws + WS_PREP + b * 4, tag);
      if (wave == 7) {
        float km = 0;
        _Pragma("unroll 1") for (int spin = 0; spin < SPIN_CAP; ++spin) {
          const u32x4 q = bld128(rs, WS_KN + vo);
          km = fmaxf(__uint_as_float(q[1]), __uint_as_float(q[3]));
          if (__all((q[0] == tag) & (q[2] == tag))) break;
          __builtin_amdgcn_s_sleep(1);
        }
        km = swap32_max(swap16_max(row_max(km)));
        if (lane == 0) lds_wf(S_KM2, km);
        _Pragma("unroll 1") for (int spin = 0; spin < SPIN_CAP; ++spin) {
          const u32x4 q = bld128(rs, WS_PREP + vo);
          if (__all((q[0] == tag) & (q[1] == tag) & (q[2] == tag) & (q[3] == tag))) break;
          __builtin_amdgcn_s_sleep(4);
        }
        ORDER_BEHIND_POLL();
        if (b == 0 && lane == 0) st4_sc1(ws + WS_EPOCH, tag);
        const u32x4 ga = bld128(rs, WS_MOD2 + vo), sb = bld128(rs, WS_MOD2 + 1024 + vo), cb = bld128(rs, WS_MOD2 + 2048 + vo), gb = bld128(rs, WS_MOD2 + 3072 + vo);
        const u32 kp = (lane >> 1) * 32 + (lane & 1) * 8;
        lds_w128(M_GA + vo, ga); lds_w128(M_GB + vo, gb);
        lds_w64(M_SB + kp, u32x2{sb[0], sb[1]}); lds_w64(M_SB + kp + 16, u32x2{sb[2], sb[3]});
        lds_w64(M_CB + kp, u32x2{cb[0], cb[1]}); lds_w64(M_CB + kp + 16, u32x2{cb[2], cb[3]});
        lds_post(S_PREP, 1);
      } else {
        lds_wait<8>(S_PREP, 1);
      }
      STAMP(9);
      u32x4 xs[4];
#pragma unroll
      for (int i = 0; i < 4; ++i) xs[i] = *(const u32x4*)(x + (64 * tg + 32 * jj + 8 * i + (lane >> 3)) * DM + 64 * ng + 8 * (lane & 7));
#pragma unroll
      for (int i = 0; i < 4; ++i) lds_w128(T_X2 + (32 * jj + 8 * i + (lane >> 3)) * 128 + (((lane & 7) ^ (lane >> 3)) * 16), xs[i]);
    }
    STAMP(10);
    {
      u32x4 wot[16];
      const u32 wi = (j & 1) * 32 + 16 * (j >> 1);
#pragma unroll
      for (int i = 0; i < 16; ++i) wot[i] = bld128(rs, WS_WOT + (2 * ng * 32 + wi + i) * 1024 + vo);
#pragma unroll
      for (int i = 0; i < 32; ++i) w[i] = bld128(rs, WS_W1T + (at * 32 + i) * 1024 + vo);
      asm volatile("s_barrier" ::: "memory");
      STAMP(11);
#pragma unroll
      for (int i = 0; i < 16; ++i) lds_w128(T_WO + (wi + i) * 1024 + vo, wot[i]);
      BAR();
      STAMP(12);
    }
    COLUMN_SUMS();
    STAMP(15);
  }

  const int rot = 2 * (cs & 1);
  const u32 ysrc = WS_Y2F + 4 * G * 32768;
  u32x4 wdr[28];
  const int cF = tg & 1, cL = 1 - cF;
  const u32 wsrc0 = WS_WDT + (2 * ng * 112 + 14 * wave + 7 * cF) * 1024 + vo, wsrc1 = WS_WDT + (2 * ng * 112 + 14 * wave + 7 * cL) * 1024 + vo;
#define LOAD_WD(I) wdr[I] = bld128(rs, ((I) < 14 ? wsrc0 + ((I) >> 1) * 1024 : wsrc1 + (((I) >> 1) - 7) * 1024) + ((I) & 1) * 112 * 1024)
  if (wave == 3) {
    u32x4 stp[8];
    const u32 stsrc = WS_ST + (2 * G + hh) * 4096 + r * 16;
    const char* y1 = ws + ysrc + ((1 + rot) & 3) * 32768;
    {
      const u32 src = ysrc + ((1 + rot) & 3) * 32768 + (lane >> 1) * 1024 + (lane & 1) * 1008;
      __builtin_amdgcn_s_sleep(Y_DELAY);
      _Pragma("unroll 1") for (int spin = 0; spin < SPIN_CAP; ++spin) {
        if (__all(__builtin_amdgcn_raw_buffer_load_b32(rs, src, 0, SC1) != EMPTY)) break;
        __builtin_amdgcn_s_sleep(1);
      }
    }
#pragma unroll
    for (int i = 0; i < 8; ++i) stp[i] = bld128(rs, stsrc + i * 512);
#pragma unroll
    for (int i = 0; i < 8; ++i) glds4_sc1(y1 + i * 4096, vo, lb + T_Y + 32768 + i * 4096);
    STAMP(16);
    BAR();
    STAMP(17);
    {
      if (__builtin_expect(!filled<8>(stp), 0)) {
        _Pragma("unroll 1") for (int spin = 0; spin < SPIN_CAP; ++spin) {
#pragma unroll
          for (int i = 0; i < 8; ++i) stp[i] = bld128(rs, stsrc + i * 512);
          if (filled<8>(stp)) break;
          __builtin_amdgcn_s_sleep(1);
        }
      }
      float m0 = 0, m1 = 0, q0 = 0, q1 = 0;
#pragma unroll
      for (int i = 0; i < 8; ++i) {
        m0 += __uint_as_float(stp[i][0]); q0 += __uint_as_float(stp[i][1]); m1 += __uint_as_float(stp[i][2]); q1 += __uint_as_float(stp[i][3]);
      }
      m0 *= 1.f / DM; m1 *= 1.f / DM;
      const float r0 = rsqrtf(fmaxf(q0 * (1.f / DM) - m0 * m0, 0.f) + 1e-5f), r1 = rsqrtf(fmaxf(q1 * (1.f / DM) - m1 * m1, 0.f) + 1e-5f);
      lds_wf4(T_ST + (64 * hh + 2 * r) * 8, f32x4{r0, m0 * r0, r1, m1 * r1});
      lds_post(S_STOK, 1);
    }
    STAMP(18);
#pragma unroll 1
    for (int tt = 1; tt < 4; ++tt) {
      const u32 dst = T_Y + tt * 32768;
      if (tt < 3) {
        const char* nsrc = ws + ysrc + ((tt + 1 + rot) & 3) * 32768;
#pragma unroll
        for (int i = 0; i < 8; ++i) glds4_sc1(nsrc + i * 4096, vo, lb + dst + 32768 + i * 4096);
        WAITV(32);
      } else {
        WAITV(0);
      }
      _Pragma("unroll 1") for (int spin = 0; spin < SPIN_CAP; ++spin) {
        u32 m = 0, m2 = 0;
#pragma unroll
        for (int h = 0; h < 2; ++h) {
          u32x4 f[16];
#pragma unroll
          for (int i = 0; i < 16; ++i) f[i] = lds_r128(dst + (16 * h + i) * 1024 + vo);
          SBAR();
#pragma unroll
          for (int i = 0; i < 16; ++i) { m = umax(umax(m, f[i][0]), f[i][1]); m2 = umax(umax(m2, f[i][2]), f[i][3]); }
        }
        if (__all(umax(m, m2) != EMPTY)) break;
        const char* src = ws + ysrc + ((tt + rot) & 3) * 32768;
#pragma unroll
        for (int i = 0; i < 8; ++i) glds4_sc1(src + i * 4096, vo, lb + dst + i * 4096);
        WAITV(0);
      }
      lds_post(S_YV, tt);
    }
    STAMP(19);
#pragma unroll
    for (int i = 0; i < 28; ++i) LOAD_WD(i);
  } else {
    if (wave >= 4) {
      u32x4 f[8];
      const u32 src = ysrc + rot * 32768 + 8 * (wave - 4) * 1024 + vo;
      __builtin_amdgcn_s_sleep(Y_DELAY2);
#pragma unroll
      for (int i = 0; i < 8; ++i) f[i] = bld128(rs, src + i * 1024);
      settle<8>(rs, f, src);
#pragma unroll
      for (int i = 0; i < 8; ++i) lds_w128(T_Y + (8 * (wave - 4) + i) * 1024 + vo, f[i]);
    }
    STAMP(16);
    BAR();
    STAMP(17);
    const u32 usa = T_US + (at - 7 * cs) * 256 + hh * 64;
#define FFN1_EPILOGUE(TT)                                                                              \
    {                                                                                                  \
      const int tile = ((TT) + rot) & 3;                                                               \
      const u32x2 st = lds_r64(T_ST + (tile * 32 + r) * 8);                                            \
      const float rsd = __uint_as_float(st[0]), mr = __uint_as_float(st[1]);                           \
      const float rg = -LOG2E * rsd, mg = LOG2E * mr;                                                  \
      u32x4 hv;                                                                                        \
      _Pragma("unroll") for (int e = 0; e < 2; ++e) {                                                  \
        const f32x4 ug = lds_rf4(usa + 16 * e), uu = lds_rf4(usa + 32 + 16 * e);                       \
        const f32x4 sg = lds_rf4(usa + 128 + 16 * e), su = lds_rf4(usa + 160 + 16 * e);                \
        float h4[4];                                                                                   \
        _Pragma("unroll") for (int j = 0; j < 4; ++j) {                                                \
          const float gt = rg * acc[4 * e + j] + (mg * ug[j] + sg[j]);                                 \
          const float up = rsd * acc[8 + 4 * e + j] + (su[j] - mr * uu[j]);                            \
          h4[j] = up * __builtin_amdgcn_rcpf(1.f + EXP2(gt));                                          \
        }                                                                                              \
        hv[2 * e] = pk2(h4[0], h4[1]); hv[2 * e + 1] = pk2(h4[2], h4[3]);                              \
      }                                                                                                \
      bst128(rs, WS_HF + ((4 * G + tile) * 112 + at) * 1024 + vo, noempty4(hv));                       \
      SBAR();                                                                                          \
    }
#define FFN1_TILE(TT, EXTRA)                                                                           \
    {                                                                                                  \
      if ((TT) > 0) lds_wait<1>(S_YV, TT);                                                             \
      f32x16 acc = {};                                                                                 \
      CHAIN32(acc, T_Y + (TT) * 32768 + vo, 1024, 6, EXTRA);                                           \
      if ((TT) == 1) lds_arrive(S_T1D, lane);                                                          \
      if ((TT) == 0) lds_wait<1>(S_STOK, 1);                                                           \
      FFN1_EPILOGUE(TT);                                                                               \
    }
    FFN1_TILE(0, if ((i & 7) == 7) LOAD_WD(i >> 3));
    STAMP(26);
    FFN1_TILE(1, if ((i & 7) == 7) LOAD_WD(4 + (i >> 3)));
    STAMP(27);
    FFN1_TILE(2, if ((i & 7) == 7) LOAD_WD(8 + (i >> 3)));
    STAMP(28);
    FFN1_TILE(3, if (i < 16) LOAD_WD(12 + i));
  }
  STAMP(20);
  {
    u32x4 hb[14], hl[7];
    const u32 h00 = WS_HF + (2 * tg * 112 + 14 * wave + 7 * cF) * 1024 + vo, h01 = WS_HF + (2 * tg * 112 + 14 * wave + 7 * cL) * 1024 + vo;
    const u32 h10 = h00 + 112 * 1024, h11 = h01 + 112 * 1024;
#pragma unroll
    for (int i = 0; i < 7; ++i) hb[i] = bld128(rs, h00 + i * 1024);
#pragma unroll
    for (int i = 0; i < 7; ++i) hb[7 + i] = bld128(rs, h01 + i * 1024);
#pragma unroll
    for (int i = 0; i < 7; ++i) hl[i] = bld128(rs, h10 + i * 1024);
    f32x16 c0 = {}, c1 = {}, d0 = {}, d1 = {};
    settle<7>(rs, hb, h00);
    STAMP(21);
#pragma unroll
    for (int i = 0; i < 7; ++i) { c0 = MFMA32(wdr[2 * i], hb[i], c0); c1 = MFMA32(wdr[2 * i + 1], hb[i], c1); }
    settle<7>(rs, hb + 7, h01);
#pragma unroll
    for (int i = 7; i < 14; ++i) { c0 = MFMA32(wdr[2 * i], hb[i], c0); c1 = MFMA32(wdr[2 * i + 1], hb[i], c1); }
    settle<7>(rs, hl, h10);
#pragma unroll
    for (int i = 0; i < 7; ++i) { d0 = MFMA32(wdr[2 * i], hl[i], d0); d1 = MFMA32(wdr[2 * i + 1], hl[i], d1); }
    SBAR();
    STAMP(22);
    __builtin_amdgcn_s_sleep(H_DELAY);
#pragma unroll
    for (int i = 0; i < 7; ++i) hl[i] = bld128(rs, h11 + i * 1024);
    SBAR();
    const int ln = fresh_lane(), r5 = ln & 31, h5 = ln >> 5;
    const u32 ro = T_RED + wave * 8192 + h5 * 512;
#define DUMP(OFF, C0, C1)                                                                              \
    _Pragma("unroll") for (int q = 0; q < 4; ++q) {                                                    \
      lds_wf4(ro + (OFF) + q * 1024 + ((r5 ^ (2 * q + h5)) * 16), f32x4{C0[4 * q], C0[4 * q + 1], C0[4 * q + 2], C0[4 * q + 3]}); \
      lds_wf4(ro + (OFF) + 4096 + q * 1024 + ((r5 ^ (8 + 2 * q + h5)) * 16), f32x4{C1[4 * q], C1[4 * q + 1], C1[4 * q + 2], C1[4 * q + 3]}); \
    }
#define REDUCE(P)                                                                                      \
    {                                                                                                  \
      const int cq = ln & 15, tk = 4 * wave + (ln >> 4);                                               \
      const u32 ra = T_RED + (P) * 65536 + (cq >> 1) * 1024 + (cq & 1) * 512 + ((tk ^ cq) * 16);       \
      f32x4 a = {0, 0, 0, 0};                                                                          \
      _Pragma("unroll") for (int v = 0; v < 8; ++v) a += lds_rf4(ra + v * 8192);                       \
      const u32x2 x2 = lds_r64(T_X2 + (32 * (P) + tk) * 128 + (((cq >> 1) ^ (tk & 7)) * 16) + (cq & 1) * 8); \
      const u32x2 gb = lds_r64(M_GB + (64 * ng + 4 * cq) * 2);                                         \
      *(u32x2*)(out + (64 * tg + 32 * (P) + tk) * DM + 64 * ng + 4 * cq) =                             \
          u32x2{pk2(lo2f(x2[0]) + lo2f(gb[0]) * a[0], hi2f(x2[0]) + hi2f(gb[0]) * a[1]),               \
                pk2(lo2f(x2[1]) + lo2f(gb[1]) * a[2], hi2f(x2[1]) + hi2f(gb[1]) * a[3])};              \
    }
    lds_wait<1>(S_T1D, 7);
    DUMP(0, c0, c1);
    BAR();
    STAMP(23);
    REDUCE(0);
    settle<7>(rs, hl, h11);
#pragma unroll
    for (int i = 0; i < 7; ++i) { d0 = MFMA32(wdr[14 + 2 * i], hl[i], d0); d1 = MFMA32(wdr[15 + 2 * i], hl[i], d1); }
    STAMP(24);
    DUMP(65536, d0, d1);
    BAR();
    REDUCE(1);
  }
  STAMPE(25);
}

extern "C" void kernel_launch(void* const* d_in, const int* in_sizes, int n_in, void* d_out, int out_size, void* d_ws, size_t ws_size,
                              hipStream_t stream) {
  (void)in_sizes; (void)n_in; (void)out_size; (void)ws_size;
  (void)hipFuncSetAttribute((const void*)fused_block, hipFuncAttributeMaxDynamicSharedMemorySize, LDS_BYTES);
  fused_block<<<256, 512, LDS_BYTES, stream>>>((const u16*)d_in[0], (const u16*)d_in[1], (const u16*)d_in[2], (const u16*)d_in[3],
                                               (const u16*)d_in[4], (const u16*)d_in[6], (const u16*)d_in[7], (const u16*)d_in[8],
                                               (const u16*)d_in[9], (const u16*)d_in[10], (char*)d_ws, (u16*)d_out);
}
```

```cpp
#include <hip/hip_runtime.h>
#include <hip/hip_bf16.h>
#include <stdint.h>

typedef unsigned short u16;
typedef unsigned int u32;
typedef unsigned long long u64;
typedef __bf16 bf16x8 __attribute__((ext_vector_type(8)));
typedef __bf16 bf16x2 __attribute__((ext_vector_type(2)));
typedef short s16x4 __attribute__((ext_vector_type(4)));
typedef float f32x16 __attribute__((ext_vector_type(16)));
typedef float f32x4 __attribute__((ext_vector_type(4)));
typedef float f32x2 __attribute__((ext_vector_type(2)));
typedef u32 u32x4 __attribute__((ext_vector_type(4)));
typedef u32 u32x2 __attribute__((ext_vector_type(2)));
#define LDSP __attribute__((address_space(3)))
#define GLBP __attribute__((address_space(1)))

constexpr int SEQ = 2048, DM = 512, FF = 1792;
constexpr float LOG2E = 1.4426950408889634f;

constexpr u32 WS_EPOCH = 0;
constexpr u32 WS_KF    = 1024;
constexpr u32 WS_PREP  = 3072;
constexpr u32 WS_MODG  = 4096;
constexpr u32 WS_YF    = 13312;
constexpr u32 WS_MOD2  = 16384;
constexpr u32 WS_FILL  = 12288;
constexpr u32 WS_ST    = 24576;
constexpr u32 WS_KV    = WS_ST + 131072;
constexpr u32 WS_WOT   = WS_KV + 131072;
constexpr u32 WS_W1T   = WS_WOT + 524288;
constexpr u32 WS_WDT   = WS_W1T + 3584 * 1024;
constexpr u32 WS_OF    = WS_WDT + 1792 * 1024;
constexpr u32 WS_Y2F   = WS_OF + 2097152;
constexpr u32 WS_HF    = WS_Y2F + 2097152;
constexpr u32 WS_END   = WS_HF + 64 * 112 * 1024;

__device__ __forceinline__ float bf2f(u32 v) { return __uint_as_float(v << 16); }
__device__ __forceinline__ float lo2f(u32 w) { return __uint_as_float(w << 16); }
__device__ __forceinline__ float hi2f(u32 w) { return __uint_as_float(w & 0xffff0000u); }
__device__ __forceinline__ u32 pk2(float a, float b) {
  f32x2 v = {a, b};
  return __builtin_bit_cast(u32, __builtin_convertvector(v, bf16x2));
}
__device__ __forceinline__ float rbf(float a) { return lo2f(pk2(a, a)); }
__device__ __forceinline__ bf16x8 asbf(u32x4 v) { return __builtin_bit_cast(bf16x8, v); }
#define MFMA32(a, b, c) __builtin_amdgcn_mfma_f32_32x32x16_bf16(asbf(a), asbf(b), c, 0, 0, 0)
#define MFMA16(a, b, c) __builtin_amdgcn_mfma_f32_16x16x32_bf16(asbf(a), asbf(b), c, 0, 0, 0)
#define SBAR() __builtin_amdgcn_sched_barrier(0)
#define PIN(x) asm volatile("" : "+v"(x))
#define BAR() asm volatile("s_waitcnt lgkmcnt(0)\n\ts_barrier" ::: "memory")
#define WAITV_(N) asm volatile("s_waitcnt vmcnt(" #N ")" ::: "memory")
#define WAITV(N) WAITV_(N)
#define WAITV_BAR(N) asm volatile("s_waitcnt vmcnt(" #N ") lgkmcnt(0)\n\ts_barrier" ::: "memory")
#define RLX_AGENT __ATOMIC_RELAXED, __HIP_MEMORY_SCOPE_AGENT
constexpr int SC1 = 16;
#define SPIN_LOG 16
constexpr int SPIN_CAP = 1 << SPIN_LOG;

template <int CTRL>
__device__ __forceinline__ float dpp(float x) {
  return __int_as_float(__builtin_amdgcn_update_dpp(0, __float_as_int(x), CTRL, 0xf, 0xf, true));
}
__device__ __forceinline__ float row_sum(float x) {
  x += dpp<0x128>(x); x += dpp<0x124>(x); x += dpp<0x122>(x); x += dpp<0x121>(x); return x;
}
__device__ __forceinline__ float row_max(float x) {
  x = fmaxf(x, dpp<0x128>(x)); x = fmaxf(x, dpp<0x124>(x)); x = fmaxf(x, dpp<0x122>(x)); x = fmaxf(x, dpp<0x121>(x)); return x;
}
__device__ __forceinline__ float swap32_add(float a, float b) {
  auto r = __builtin_amdgcn_permlane32_swap(__float_as_uint(a), __float_as_uint(b), false, false);
  return __uint_as_float(r[0]) + __uint_as_float(r[1]);
}
__device__ __forceinline__ float swap16_add(float a, float b) {
  auto r = __builtin_amdgcn_permlane16_swap(__float_as_uint(a), __float_as_uint(b), false, false);
  return __uint_as_float(r[0]) + __uint_as_float(r[1]);
}
__device__ __forceinline__ float swap32_sum(float x) { return swap32_add(x, x); }
__device__ __forceinline__ float swap16_sum(float x) { return swap16_add(x, x); }
__device__ __forceinline__ float swap32_max(float x) {
  auto r = __builtin_amdgcn_permlane32_swap(__float_as_uint(x), __float_as_uint(x), false, false);
  return fmaxf(__uint_as_float(r[0]), __uint_as_float(r[1]));
}
__device__ __forceinline__ float swap16_max(float x) {
  auto r = __builtin_amdgcn_permlane16_swap(__float_as_uint(x), __float_as_uint(x), false, false);
  return fmaxf(__uint_as_float(r[0]), __uint_as_float(r[1]));
}
__device__ __forceinline__ void reduce8(const float (&v)[8], float& r0, float& r1) {
  const float a0 = swap32_add(v[0], v[1]), a1 = swap32_add(v[2], v[3]), a2 = swap32_add(v[4], v[5]), a3 = swap32_add(v[6], v[7]);
  r0 = row_sum(swap16_add(a0, a1));
  r1 = row_sum(swap16_add(a2, a3));
}
__device__ __forceinline__ void reduce8q(const float (&v)[8], float& r0, float& r1) {
  const float a0 = swap32_add(v[0], v[1]), a1 = swap32_add(v[2], v[3]), a2 = swap32_add(v[4], v[5]), a3 = swap32_add(v[6], v[7]);
  r0 = swap16_add(a0, a1); r1 = swap16_add(a2, a3);
  r0 += dpp<0x128>(r0); r0 += dpp<0x124>(r0); r1 += dpp<0x128>(r1); r1 += dpp<0x124>(r1);
}
__device__ __forceinline__ float rdlane(float x, int l) { return __int_as_float(__builtin_amdgcn_readlane(__float_as_int(x), l)); }
#define RED8_LANE(i) (16 * ((((i) & 1) << 1) | (((i) >> 1) & 1)))

template <int V> struct ic { static constexpr int value = V; constexpr operator int() const { return V; } };
template <int N, int I = 0, class F>
__device__ __forceinline__ void static_for(F&& f) { if constexpr (I < N) { f(ic<I>{}); static_for<N, I + 1>(f); } }

extern __shared__ __attribute__((aligned(16))) char smem[];
__device__ __forceinline__ u32x4 lds_r128(u32 off) { return *(const LDSP u32x4*)((LDSP char*)smem + off); }
__device__ __forceinline__ void lds_w128(u32 off, u32x4 v) { *(LDSP u32x4*)((LDSP char*)smem + off) = v; }
__device__ __forceinline__ f32x4 lds_rf4(u32 off) { return *(const LDSP f32x4*)((LDSP char*)smem + off); }
__device__ __forceinline__ void lds_wf4(u32 off, f32x4 v) { *(LDSP f32x4*)((LDSP char*)smem + off) = v; }
__device__ __forceinline__ float lds_rf(u32 off) { return *(const LDSP float*)((LDSP char*)smem + off); }
__device__ __forceinline__ void lds_wf(u32 off, float v) { *(LDSP float*)((LDSP char*)smem + off) = v; }
__device__ __forceinline__ u32x2 lds_r64(u32 off) { return *(const LDSP u32x2*)((LDSP char*)smem + off); }
__device__ __forceinline__ void lds_w64(u32 off, u32x2 v) { *(LDSP u32x2*)((LDSP char*)smem + off) = v; }
__device__ __forceinline__ void lds_w32(u32 off, u32 v) { *(LDSP u32*)((LDSP char*)smem + off) = v; }
__device__ __forceinline__ u32 lds_r32(u32 off) { return *(const LDSP u32*)((LDSP char*)smem + off); }
__device__ __forceinline__ u32 lds_base() { return (u32)(uintptr_t)(LDSP char*)smem; }
typedef short v4i16_t __attribute__((ext_vector_type(4)));
__device__ __forceinline__ u32x2 lds_tr(u32 off) {
  return __builtin_bit_cast(u32x2, __builtin_amdgcn_ds_read_tr16_b64_v4i16((LDSP v4i16_t*)((LDSP char*)smem + off)));
}
__device__ __forceinline__ u32x4 lds_tr2(u32 off, u32 step) {
  const u32x2 a = lds_tr(off), c = lds_tr(off + step);
  return u32x4{a[0], a[1], c[0], c[1]};
}

#define GLDS_ASM(NAME, BODY)                                                                             \
  __device__ __forceinline__ void NAME(const void* g, u32 voff, u32 lds) {                               \
    u32 sv;                                                                                              \
    asm volatile("s_mov_b32 %0, m0\n\ts_mov_b32 m0, %3\n\ts_nop 0\n\t" BODY "s_mov_b32 m0, %0"           \
                 : "=&s"(sv) : "v"(voff), "s"(g), "s"(lds) : "memory");                                  \
  }
GLDS_ASM(glds1, "global_load_lds_dwordx4 %1, %2\n\t")
GLDS_ASM(glds1_sc1, "global_load_lds_dwordx4 %1, %2 sc1\n\t")
GLDS_ASM(glds4_sc1, "global_load_lds_dwordx4 %1, %2 sc1\n\tglobal_load_lds_dwordx4 %1, %2 offset:1024 sc1\n\t"
                    "global_load_lds_dwordx4 %1, %2 offset:2048 sc1\n\tglobal_load_lds_dwordx4 %1, %2 offset:3072 sc1\n\t")
#undef GLDS_ASM

__device__ __forceinline__ u32 ld4_sc1(const void* p) { return __hip_atomic_load((GLBP u32*)p, RLX_AGENT); }
__device__ __forceinline__ void st4_sc1(void* p, u32 v) { __hip_atomic_store((GLBP u32*)p, v, RLX_AGENT); }
__device__ __forceinline__ void st8_sc1(void* p, u32 lo, u32 hi) {
  __hip_atomic_store((GLBP u64*)p, ((u64)hi << 32) | lo, RLX_AGENT);
}
typedef __amdgpu_buffer_rsrc_t rsrc_t;
__device__ __forceinline__ u32x4 bld128(rsrc_t rs, u32 off) { return __builtin_amdgcn_raw_buffer_load_b128(rs, off, 0, SC1); }
__device__ __forceinline__ u32x2 bld64(rsrc_t rs, u32 off) { return __builtin_amdgcn_raw_buffer_load_b64(rs, off, 0, SC1); }
__device__ __forceinline__ void bst128(rsrc_t rs, u32 off, u32x4 v) { __builtin_amdgcn_raw_buffer_store_b128(v, rs, off, 0, SC1); }
__device__ __forceinline__ void bst64(rsrc_t rs, u32 off, u32x2 v) { __builtin_amdgcn_raw_buffer_store_b64(v, rs, off, 0, SC1); }
__device__ __forceinline__ u32x4 bld128s(rsrc_t rs, u32 voff, u32 soff) { return __builtin_amdgcn_raw_buffer_load_b128(rs, voff, soff, SC1); }
__device__ __forceinline__ void bst128s(rsrc_t rs, u32 voff, u32 soff, u32x4 v) { __builtin_amdgcn_raw_buffer_store_b128(v, rs, voff, soff, SC1); }
#define ORDER_BEHIND_POLL() __builtin_amdgcn_fence(__ATOMIC_ACQUIRE, "wavefront")

extern __shared__ __attribute__((aligned(16))) u32 smem32[];
__device__ __forceinline__ u32 lds_peek(u32 off) {
  return __hip_atomic_load((LDSP u32*)smem32 + off / 4, __ATOMIC_RELAXED, __HIP_MEMORY_SCOPE_WORKGROUP);
}
__device__ __forceinline__ void lds_post(u32 off, u32 v) {
  asm volatile("" ::: "memory");
  __hip_atomic_store((LDSP u32*)smem32 + off / 4, v, __ATOMIC_RELAXED, __HIP_MEMORY_SCOPE_WORKGROUP);
}
__device__ __forceinline__ u32 lds_arrive(u32 off, int lane) {
  asm volatile("" ::: "memory");
  u32 old = 0;
  if (lane == 0) old = __hip_atomic_fetch_add((LDSP u32*)smem32 + off / 4, 1u, __ATOMIC_RELAXED, __HIP_MEMORY_SCOPE_WORKGROUP);
  old = __builtin_amdgcn_readfirstlane(old);
  asm volatile("" ::: "memory");
  return old;
}
template <int SLEEP>
__device__ __forceinline__ void lds_wait(u32 off, u32 v) {
  _Pragma("unroll 1") for (int spin = 0; spin < SPIN_CAP; ++spin) {
    if ((u32)__builtin_amdgcn_readfirstlane(lds_peek(off)) >= v) break;
    __builtin_amdgcn_s_sleep(SLEEP);
  }
  asm volatile("" ::: "memory");
}
__device__ __forceinline__ int fresh_lane() {
  int l; asm volatile("v_mbcnt_lo_u32_b32 %0, -1, 0\n\tv_mbcnt_hi_u32_b32 %0, -1, %0" : "=v"(l)); return l;
}
constexpr u32 EMPTY = 0xffffffffu;
__device__ __forceinline__ u32 umax(u32 a, u32 b) { return a > b ? a : b; }
__device__ __forceinline__ u32 noempty(u32 v) { return v < EMPTY ? v : EMPTY - 1; }
__device__ __forceinline__ u32x4 noempty4(u32x4 v) { return u32x4{noempty(v[0]), noempty(v[1]), noempty(v[2]), noempty(v[3])}; }
template <int N>
__device__ __forceinline__ bool filled(const u32x4* f) {
  u32 m = 0, m2 = 0;
#pragma unroll
  for (int i = 0; i < N; ++i) { m = umax(umax(m, f[i][0]), f[i][1]); m2 = umax(umax(m2, f[i][2]), f[i][3]); }
  return __all(umax(m, m2) != EMPTY);
}
template <int N>
__device__ __forceinline__ void settle(rsrc_t rs, u32x4* f, u32 off) {
  if (__builtin_expect(!filled<N>(f), 0)) {
    _Pragma("unroll 1") for (int spin = 0; spin < SPIN_CAP; ++spin) {
#pragma unroll
      for (int i = 0; i < N; ++i) f[i] = bld128(rs, off + i * 1024);
      if (filled<N>(f)) break;
      __builtin_amdgcn_s_sleep(1);
    }
  }
}

template <int N>
__device__ __forceinline__ void settle_s(rsrc_t rs, u32x4* f, u32 voff, u32 soff) {
  if (__builtin_expect(!filled<N>(f), 0)) {
    _Pragma("unroll 1") for (int spin = 0; spin < SPIN_CAP; ++spin) {
#pragma unroll
      for (int i = 0; i < N; ++i) f[i] = bld128s(rs, voff, soff + i * 1024);
      if (filled<N>(f)) break;
      __builtin_amdgcn_s_sleep(1);
    }
  }
}

#define STAMP_DECL()
#define STAMP(I)
#define STAMPE(I)
#define KV_DELAY 12
#define O_DELAY 10
#define Y_DELAY 10
#define Y_DELAY2 20
#define H_DELAY 1

constexpr u32 P_WQ = 0, P_H = 65536, P_WKV = 131072, P_M1 = 147456, P_PART = 149504;
constexpr u32 A_KV = 0, H_STG = 131072;
constexpr u32 T_WO = 65536, T_Y = 0, T_RED = 0, T_XCH = 131072, T_X2 = 147456,
              T_STX = 155648, T_US = 157696, T_BX = T_XCH, T_RS = T_XCH + 2048;
constexpr u32 L_ONES = 155648, L_SYNC = 159616, L_MODI = 159744, LDS_BYTES = 163840;
constexpr u32 S_PROG = L_SYNC, S_CONS = L_SYNC + 16, S_PARR = L_SYNC + 20, S_PREP = L_SYNC + 24, S_STARR = L_SYNC + 28,
              S_YV = L_SYNC + 32, S_STOK = L_SYNC + 36, S_KM2 = L_SYNC + 64, S_T1D = L_SYNC + 44, S_XCH = L_SYNC + 68;
constexpr u32 S_T2D = L_SYNC + 48, S_T3D = L_SYNC + 52, S_Y0 = L_SYNC + 56, S_YL = L_SYNC + 84, S_Y1 = L_SYNC + 88, S_T0D = L_SYNC + 92;
constexpr u32 S_FARR = L_SYNC + 8, S_FILL = L_SYNC + 12, S_HW = L_SYNC + 40, S_AW = L_SYNC + 60;
constexpr u32 M_GA = L_MODI, M_SB = L_MODI + 1024, M_CB = L_MODI + 2048, M_GB = L_MODI + 3072, M_SILU = L_MODI;
constexpr float QSCALE = 0.25f * LOG2E;
constexpr float BOUND2 = 64.f * 64.f;
#define EXP2(x) __builtin_amdgcn_exp2f(x)

__device__ __forceinline__ u32x4 ald128(const void* g, u32 voff) {
  u32x4 v; asm volatile("global_load_dwordx4 %0, %1, %2" : "=v"(v) : "v"(voff), "s"(g) : "memory"); return v;
}
__device__ __forceinline__ u32x2 ald64(const void* g, u32 voff) {
  u32x2 v; asm volatile("global_load_dwordx2 %0, %1, %2" : "=v"(v) : "v"(voff), "s"(g) : "memory"); return v;
}
__device__ __forceinline__ u32 ald16(const void* g, u32 voff) {
  u32 v; asm volatile("global_load_ushort %0, %1, %2" : "=v"(v) : "v"(voff), "s"(g) : "memory"); return v;
}
__device__ __forceinline__ u32 ald32_sc1(const void* g, u32 voff) {
  u32 v; asm volatile("global_load_dword %0, %1, %2 sc1" : "=v"(v) : "v"(voff), "s"(g) : "memory"); return v;
}
__device__ __forceinline__ void glds1_v(const void* p, u32 lds) {
  u32 sv;
  asm volatile("s_mov_b32 %0, m0\n\ts_mov_b32 m0, %2\n\ts_nop 0\n\tglobal_load_lds_dwordx4 %1, off\n\ts_mov_b32 m0, %0"
               : "=&s"(sv) : "v"(p), "s"(lds) : "memory");
}

__device__ __forceinline__ void pf_line(const void* g, u32 voff, u32 lds) {
  u32 sv;
  asm volatile("s_mov_b32 %0, m0\n\ts_mov_b32 m0, %3\n\ts_nop 0\n\tglobal_load_lds_dword %1, %2 sc1\n\ts_mov_b32 m0, %0"
               : "=&s"(sv) : "v"(voff), "s"(g), "s"(lds) : "memory");
}
#define W1_AT 24
#define HW_WAIT 8
#define AW_GATE 4
#define RAW_GATE 1
#define PT_GATE 8
#define FILL_GATE 0
#define W_LATE 4
#define H_PRIO 2
#define HALF(Sc, Sn, Kn, Qn, Oc, V0, V1, CIN)                                                          \
  do {                                                                                                 \
    u32x4 p0_, p1_;                                                                                    \
    Sc[0] = EXP2(Sc[0]); Sc[1] = EXP2(Sc[1]); Sc[2] = EXP2(Sc[2]); Sc[3] = EXP2(Sc[3]);                \
    p0_[0] = pk2(Sc[0], Sc[1]); p0_[1] = pk2(Sc[2], Sc[3]); PIN(p0_[0]); PIN(p0_[1]); SBAR();          \
    Sn = MFMA32(Kn, Qn, CIN); SBAR();                                                                  \
    Sc[4] = EXP2(Sc[4]); Sc[5] = EXP2(Sc[5]); Sc[6] = EXP2(Sc[6]); Sc[7] = EXP2(Sc[7]);                \
    p0_[2] = pk2(Sc[4], Sc[5]); p0_[3] = pk2(Sc[6], Sc[7]); PIN(p0_); SBAR();                          \
    Oc = MFMA32(V0, p0_, Oc); SBAR();                                                                  \
    Sc[8] = EXP2(Sc[8]); Sc[9] = EXP2(Sc[9]); Sc[10] = EXP2(Sc[10]); Sc[11] = EXP2(Sc[11]);            \
    p1_[0] = pk2(Sc[8], Sc[9]); p1_[1] = pk2(Sc[10], Sc[11]); PIN(p1_[0]); PIN(p1_[1]); SBAR();        \
    Sc[12] = EXP2(Sc[12]); Sc[13] = EXP2(Sc[13]); Sc[14] = EXP2(Sc[14]); Sc[15] = EXP2(Sc[15]);        \
    p1_[2] = pk2(Sc[12], Sc[13]); p1_[3] = pk2(Sc[14], Sc[15]); PIN(p1_); SBAR();                      \
    Oc = MFMA32(V1, p1_, Oc); SBAR();                                                                  \
  } while (0)
#define TWO_TILES(X0, X1, X2, X3)                                                                                    \
  do {                                                                                                 \
    const u32 ka = A_KV + t * 2048 + vo;                                                               \
    u32x4 Kn = lds_r128(ka + 2048);                                                                    \
    u32x4 W0 = lds_r128(vadr + 2048), W1 = lds_r128(vadr + 2560);                                      \
    SBAR();                                                                                            \
    HALF(Sa, Sb, Kc, Qb, Oa, V0, V1, zero);                                                            \
    X0; SBAR();                                                                                        \
    HALF(Sb, Sa, Kn, Qa, Ob, V0, V1, zero);                                                            \
    X1; SBAR();                                                                                        \
    vadr += 2 * vstep;                                                                                 \
    Kc = lds_r128(ka + 4096);                                                                          \
    V0 = lds_r128(vadr); V1 = lds_r128(vadr + 512);                                                    \
    SBAR();                                                                                            \
    HALF(Sa, Sb, Kn, Qb, Oa, W0, W1, zero);                                                            \
    X2; SBAR();                                                                                        \
    HALF(Sb, Sa, Kc, Qa, Ob, W0, W1, zero);                                                            \
    X3; SBAR();                                                                                        \
  } while (0)
__device__ __forceinline__ void wait_kv(u32 n) {
  _Pragma("unroll 1") for (int spin = 0; spin < SPIN_CAP; ++spin) {
    asm volatile("" ::: "memory");
    const u32x2 p = lds_r64(S_PROG);
    if ((u32)__builtin_amdgcn_readfirstlane(p[0] < p[1] ? p[0] : p[1]) >= n) break;
    __builtin_amdgcn_s_sleep(1);
  }
  asm volatile("" ::: "memory");
}
#define CHAIN32(ACC, ADDR, STRIDE, DEPTH, EXTRA)                                                       \
  {                                                                                                    \
    u32x4 ring_[DEPTH];                                                                                \
    _Pragma("unroll") for (int i = 0; i < DEPTH; ++i) ring_[i] = lds_r128((ADDR) + i * (STRIDE));      \
    SBAR();                                                                                            \
    _Pragma("unroll") for (int i = 0; i < 32; ++i) {                                                   \
      ACC = MFMA32(w[i], ring_[i % DEPTH], ACC);                                                       \
      if (i + DEPTH < 32) ring_[i % DEPTH] = lds_r128((ADDR) + (i + DEPTH) * (STRIDE));                \
      EXTRA;                                                                                           \
      SBAR();                                                                                          \
    }                                                                                                  \
  }
#define COLUMN_SUMS()                                                                                  \
  {                                                                                                    \
    f32x16 us = {};                                                                                    \
    const int ln_ = fresh_lane();                                                                      \
    const u32 ma = ((ln_ & 1) ? M_SB : M_CB) + (ln_ >> 5) * 16;                                        \
    CHAIN32(us, ma, 32, 8, );                                                                          \
    const u32 ub = T_US + (at - 7 * cs) * 256;                                                         \
    if ((ln_ & 31) < 2) {                                                                              \
      _Pragma("unroll") for (int q = 0; q < 4; ++q)                                                    \
        lds_wf4(ub + (ln_ & 1) * 128 + (ln_ >> 5) * 16 + q * 32, f32x4{us[4 * q], us[4 * q + 1], us[4 * q + 2], us[4 * q + 3]}); \
    }                                                                                                  \
    const float u_ = lds_rf(ub + (ln_ & 31) * 4), s_ = lds_rf(ub + 128 + (ln_ & 31) * 4);              \
    const float uh = rbf(u_), sh = rbf(s_);                                                            \
    wX = u32x4{pk2(uh, uh), pk2(u_ - uh, sh), pk2(sh, s_ - sh), 0u};                                   \
    if (ln_ >= 32) wX = u32x4{0u, 0u, 0u, 0u};                                                         \
  }

__global__ __launch_bounds__(512, 2) void fused_block(const u16* __restrict__ x, const u16* __restrict__ wq, const u16* __restrict__ wk,
                                                      const u16* __restrict__ wv, const u16* __restrict__ wo, const u16* __restrict__ wg,
                                                      const u16* __restrict__ wu, const u16* __restrict__ wd, const u16* __restrict__ cvec,
                                                      const u16* __restrict__ wmod, char* __restrict__ ws, u16* __restrict__ out) {
  const int tid = threadIdx.x, lane = tid & 63, b = blockIdx.x;
  const int wave = __builtin_amdgcn_readfirstlane(tid >> 6);
  const int r = lane & 31, hh = lane >> 5, g = lane >> 4, li = lane & 15;
  const int xcd = b & 7, idx = b >> 3;
  const int tg = xcd * 4 + (idx >> 3), hg = idx & 7;
  const int ng = hg, G = 4 * (xcd >> 1) + (idx >> 3), cs = 8 * (xcd & 1) + (idx & 7);
  const u32 lb = lds_base(), vo = lane * 16;
  const rsrc_t rs = __builtin_amdgcn_make_buffer_rsrc((void*)ws, 0, WS_END, 0x00020000);
  const int tq_ = li >> 2, tp_ = lane & 3;
  STAMP_DECL();

  const int colA = 32 * (4 * xcd + idx);
  const int rowA = wave * 64 + (lane >> 2) * 4;
  u32 ev = ald32_sc1(ws + WS_EPOCH, 0);
  u32 cv = ald16(cvec, tid * 2);
  u32x4 wA[4];
#pragma unroll
  for (int i = 0; i < 4; ++i) wA[i] = ald128(wmod, idx < 4 ? (rowA + i) * 6144 + colA * 2 + (lane & 3) * 16 : 0);
  if (idx < 4) asm volatile("s_barrier" ::: "memory");
  const int kq = hg >> 1;
  auto row_of = [&](int i) { return i < 2 ? 16 * kq + 2 * wave + i : (16 * kq + 14 + 6 * wave + i) & 63; };
  u32x4 xr[8];
#pragma unroll
  for (int i = 0; i < 8; ++i) xr[i] = ald128(x + (64 * tg + row_of(i)) * DM, vo);
  lds_w32(L_ONES + tid * 4, 0x3f803f80u);
  if (tid < 256) lds_w32(L_ONES + 2048 + tid * 4, 0x3f803f80u);
  if (tid < 32) lds_w32(L_SYNC + tid * 4, 0u);
  asm volatile("s_waitcnt vmcnt(8)" : "+v"(ev), "+v"(cv), "+v"(wA[0]), "+v"(wA[1]), "+v"(wA[2]), "+v"(wA[3])::"memory");
  const u32 tag = __builtin_amdgcn_readfirstlane(ev) + 1;
  STAMP(50);
  {
    const float f = bf2f(cv), sl = rbf(f * __builtin_amdgcn_rcpf(1.f + EXP2(-LOG2E * f)));
    *(LDSP u16*)((LDSP char*)smem + M_SILU + tid * 2) = (u16)(__float_as_uint(sl) >> 16);
    if (idx < 4) {
      float a[8] = {0, 0, 0, 0, 0, 0, 0, 0}, r0, r1;
      const int sub = ((g & 1) << 1) | (g >> 1);
#pragma unroll
      for (int i = 0; i < 4; ++i) {
        const float si = i == 0 ? dpp<0x00>(sl) : i == 1 ? dpp<0x55>(sl) : i == 2 ? dpp<0xAA>(sl) : dpp<0xFF>(sl);
#pragma unroll
        for (int j = 0; j < 4; ++j) { a[2 * j] += si * lo2f(wA[i][j]); a[2 * j + 1] += si * hi2f(wA[i][j]); }
      }
      reduce8q(a, r0, r1);
      if (li < 4) { lds_wf(P_PART + (wave * 32 + li * 8 + sub) * 4, r0); lds_wf(P_PART + (wave * 32 + li * 8 + 4 + sub) * 4, r1); }
      BAR();
      if (wave == 0) {
        const int jl = lane & 31;
        float s = 0;
#pragma unroll
        for (int w = 0; w < 8; ++w) s += lds_rf(P_PART + (w * 32 + jl) * 4);
        s = rbf(s);
        const int col = colA + jl;
        if (col >= 512) s = rbf(1.f + s);
        const float sn = dpp<0xB1>(s);
        if (lane < 32 && !(lane & 1)) st8_sc1(ws + WS_MODG + (col >> 1) * 8, tag, pk2(s, sn));
      }
    }
  }
  STAMP(1);
  float xh[8][8];
  asm volatile("s_waitcnt vmcnt(0)"
               : "+v"(xr[0]), "+v"(xr[1]), "+v"(xr[2]), "+v"(xr[3]), "+v"(xr[4]), "+v"(xr[5]), "+v"(xr[6]), "+v"(xr[7])::"memory");
  if (wave == 7) {
    const u16* wkv = (hg & 1) ? wv : wk;
#pragma unroll
    for (int i = 0; i < 16; ++i) glds1(wkv + i * 512, vo, lb + P_WKV + i * 1024);
  }
  STAMP(2);
  {
    float s[8], r0, r1;
#pragma unroll
    for (int i = 0; i < 8; ++i) {
#pragma unroll
      for (int j = 0; j < 4; ++j) { xh[i][2 * j] = lo2f(xr[i][j]); xh[i][2 * j + 1] = hi2f(xr[i][j]); }
      s[i] = ((xh[i][0] + xh[i][1]) + (xh[i][2] + xh[i][3])) + ((xh[i][4] + xh[i][5]) + (xh[i][6] + xh[i][7]));
    }
    reduce8(s, r0, r1);
#pragma unroll
    for (int i = 0; i < 8; ++i) {
      const float m = rdlane(i < 4 ? r0 : r1, RED8_LANE(i)) * (1.f / DM);
      float q = 0;
#pragma unroll
      for (int j = 0; j < 8; ++j) { xh[i][j] -= m; q += xh[i][j] * xh[i][j]; }
      s[i] = q;
    }
    reduce8(s, r0, r1);
    r0 = rsqrtf(r0 * (1.f / DM) + 1e-5f); r1 = rsqrtf(r1 * (1.f / DM) + 1e-5f);
#pragma unroll
    for (int i = 0; i < 8; ++i) {
      const float rstd = rdlane(i < 4 ? r0 : r1, RED8_LANE(i));
#pragma unroll
      for (int j = 0; j < 8; ++j) xh[i][j] *= rstd;
    }
  }
  STAMP(3);
  if (wave < 4) {
    const u32 vq = (lane >> 3) * 1024 + hg * 128 + (((lane & 7) ^ (((lane >> 4) & 1) << 2)) * 16);
#pragma unroll
    for (int i = 0; i < 16; ++i) glds1(wq + (128 * wave + 8 * i) * DM, vq, lb + P_WQ + (128 * wave + 8 * i) * 128);
  }
  u32x4 wf[16];
  if (wave == 6) {
    _Pragma("unroll 1") for (int spin = 0; spin < SPIN_CAP; ++spin) {
      u32x4 q[4];
#pragma unroll
      for (int i = 0; i < 4; ++i) q[i] = bld128(rs, WS_MODG + i * 1024 + vo);
      bool ok = true;
#pragma unroll
      for (int i = 0; i < 4; ++i) { ok &= (q[i][0] == tag) & (q[i][2] == tag); lds_w64(P_M1 + i * 512 + lane * 8, u32x2{q[i][1], q[i][3]}); }
      if (__all(ok)) break;
      __builtin_amdgcn_s_sleep(1);
    }
  } else if (wave == 7) {
    WAITV(0);
    const u32 wa = P_WKV + (8 * g + tq_) * 32 + 8 * tp_;
#pragma unroll
    for (int t = 0; t < 16; ++t) wf[t] = lds_tr2(wa + t * 1024, 128);
  }
  BAR();
  STAMP(4);
  float kn2 = 0;
  {
    const u32x4 sa = lds_r128(P_M1 + vo), ca = lds_r128(P_M1 + 1024 + vo);
    float sf[8], cf[8];
#pragma unroll
    for (int j = 0; j < 4; ++j) { sf[2 * j] = lo2f(sa[j]); sf[2 * j + 1] = hi2f(sa[j]); cf[2 * j] = lo2f(ca[j]); cf[2 * j + 1] = hi2f(ca[j]); }
    auto put_row = [&](int i) {
      const int rho = row_of(i), rr = rho & 31;
      u32x4 hv;
#pragma unroll
      for (int j = 0; j < 4; ++j) hv[j] = pk2(xh[i][2 * j] * cf[2 * j] + sf[2 * j], xh[i][2 * j + 1] * cf[2 * j + 1] + sf[2 * j + 1]);
      lds_w128(P_H + (rho >> 5) * 32768 + (lane >> 1) * 1024 + (lane & 1) * 512 + ((rr ^ li) * 16), hv);
    };
    put_row(0); put_row(1);
    BAR();
    if (wave == 7) {
      __builtin_amdgcn_s_setprio(3);
      const int q = kq, m = hg & 1;
      const int rr = (q & 1) * 16 + li;
      f32x4 d0 = {0, 0, 0, 0}, d1 = {0, 0, 0, 0};
      const u32 ha = P_H + (q >> 1) * 32768 + (g >> 1) * 1024 + (g & 1) * 512;
      u32x4 hf[16];
#pragma unroll
      for (int t = 0; t < 16; ++t) hf[t] = lds_r128(ha + t * 2048 + ((rr ^ ((4 * t + g) & 15)) * 16));
      SBAR();
      if (m == 0) {
#pragma unroll
        for (int t = 0; t < 16; t += 2) { d0 = MFMA16(wf[t], hf[t], d0); d1 = MFMA16(wf[t + 1], hf[t + 1], d1); }
      } else {
#pragma unroll
        for (int t = 0; t < 16; t += 2) { d0 = MFMA16(hf[t], wf[t], d0); d1 = MFMA16(hf[t + 1], wf[t + 1], d1); }
      }
      d0 += d1;
      const u32x2 pkd = {pk2(d0[0], d0[1]), pk2(d0[2], d0[3])};
      const u32 dst = WS_KV + (2 * tg + (q >> 1)) * 2048 + (g >> 1) * 8;
      if (m == 0) {
        bst64(rs, dst + ((g & 1) * 32 + rr) * 16, pkd);
        const float k0 = lo2f(pkd[0]), k1 = hi2f(pkd[0]), k2 = lo2f(pkd[1]), k3 = hi2f(pkd[1]);
        kn2 = row_max(swap32_sum(swap16_sum(k0 * k0 + k1 * k1 + k2 * k2 + k3 * k3)));
      } else {
        bst64(rs, dst + 1024 + ((q & 1) * 32 + (g & 1) * 16 + li) * 16, pkd);
      }
      __builtin_amdgcn_s_setprio(0);
      STAMP(35);
    }
#pragma unroll
    for (int i = 2; i < 8; ++i) put_row(i);
  }
  if (wave < 4) WAITV(0);
  BAR();
  STAMP(5);
  if (wave == 7) {
    WAITV(0);
    if (lane == 0) st8_sc1(ws + WS_KF + (tg * 8 + hg) * 8, tag, __float_as_uint(kn2));
    STAMP(36);
  }

  u32x4 w[32], wX;
  const int at = 7 * cs + (wave < 4 ? 4 + wave : wave - 4);
  if (wave < 4) {
    const int th = wave & 1, hp = wave >> 1;
    u32x4 Qa, Qb; float qn2;
    {
      f32x16 qa = {};
      const u32 a0 = P_WQ + (8 * (g >> 1) + tq_) * 128 + (((4 * hp + 2 * (g & 1) + (tp_ >> 1)) ^ ((tq_ >> 1) << 2)) * 16) + 8 * (tp_ & 1);
      u32x4 ra[4], rb[4];
      auto rdq = [&](int ks) {
        ra[ks & 3] = lds_tr2(a0 + ks * 2048, 512);
        rb[ks & 3] = lds_r128(P_H + th * 32768 + ks * 1024 + hh * 512 + ((r ^ ((2 * ks + hh) & 15)) * 16));
      };
#pragma unroll
      for (int ks = 0; ks < 4; ++ks) rdq(ks);
      SBAR();
#pragma unroll
      for (int ks = 0; ks < 32; ++ks) {
        qa = MFMA32(ra[ks & 3], rb[ks & 3], qa);
        if (ks + 4 < 32) rdq(ks + 4);
        SBAR();
      }
      lds_arrive(S_CONS, lane);
#pragma unroll
      for (int j = 0; j < 4; ++j) {
        Qa[j] = pk2(qa[2 * j] * QSCALE, qa[2 * j + 1] * QSCALE); Qb[j] = pk2(qa[8 + 2 * j] * QSCALE, qa[9 + 2 * j] * QSCALE);
      }
      float na = 0, nb = 0;
#pragma unroll
      for (int j = 0; j < 4; ++j) {
        na += lo2f(Qa[j]) * lo2f(Qa[j]) + hi2f(Qa[j]) * hi2f(Qa[j]);
        nb += lo2f(Qb[j]) * lo2f(Qb[j]) + hi2f(Qb[j]) * hi2f(Qb[j]);
      }
      qn2 = fmaxf(swap32_sum(na), swap32_sum(nb));
    }
    STAMP(6);
    f32x16 Oa = {}, Ob = {};
    const u32 vadr0 = (r < 16) ? (A_KV + 1024 + hh * 256 + r * 16) : (L_ONES + ((r >= 20 && r < 28) ? 64 : 0));
    const u32 vstep = (r < 16) ? 2048u : 0u;
    {
      f32x16 Sa, Sb;
      const f32x16 zero = {};
      u32 vadr = vadr0;
      wait_kv(1);
      __builtin_amdgcn_s_setprio(1);
      STAMP(7);
      u32x4 Kc = lds_r128(A_KV + vo);
      Sa = MFMA32(Kc, Qa, zero);
      u32x4 V0 = lds_r128(vadr), V1 = lds_r128(vadr + 512);
      int t = 0;
#pragma unroll 1
      for (int seg = 0; seg < 3; ++seg) {
        const int tend = seg == 0 ? 6 : seg == 1 ? 14 : W1_AT;
#pragma unroll 1
        for (; t < tend; t += 2) TWO_TILES(, , , );
        if (seg == 0) STAMP(38); else if (seg == 1) STAMP(40); else STAMP(42);
        if (seg < 2) wait_kv(seg == 0 ? 2 : 8);
        if (seg == 0) STAMP(39); else if (seg == 1) STAMP(41);
      }
      {
        u32 seen = 0;
#pragma unroll 1
        for (; t < 56 && seen < 4; t += 2) {
          const u32 v = lds_peek(S_HW);
          TWO_TILES(, , , );
          seen = __builtin_amdgcn_readfirstlane(v);
        }
      }
      STAMP(8);
      lds_wait<1>(S_PREP, 1);
      STAMP(34);
#define W1_REQ(I) if (wave < 3 && (I) < 32 - W_LATE) { w[I] = bld128s(rs, vo, WS_W1T + (at * 32 + (I)) * 1024); w[(I) + 1] = bld128s(rs, vo, WS_W1T + (at * 32 + (I) + 1) * 1024); }
#pragma unroll
      for (int u = 0; u < 4; ++u) {
        TWO_TILES(W1_REQ(8 * u), W1_REQ(8 * u + 2), W1_REQ(8 * u + 4), W1_REQ(8 * u + 6));
        t += 2;
      }
      lds_arrive(S_AW, lane);
      STAMP(44);
#pragma unroll 1
      for (; t < 64; t += 2) TWO_TILES(, , , );
    }
    __builtin_amdgcn_s_setprio(0);
    STAMP(9);
    if (__any(!(qn2 * lds_rf(S_KM2) <= BOUND2))) {
#pragma unroll 1
      for (int x2 = 0; x2 < 2; ++x2) {
        const u32x4 Q = x2 ? Qb : Qa;
        float m = -3.0e38f;
        const f32x16 zero = {};
#pragma unroll 1
        for (int t = 0; t < 64; ++t) {
          const f32x16 s = MFMA32(lds_r128(A_KV + t * 2048 + vo), Q, zero);
#pragma unroll
          for (int j = 0; j < 16; ++j) m = fmaxf(m, s[j]);
        }
        m = swap32_max(m);
        f32x16 cm;
#pragma unroll
        for (int j = 0; j < 16; ++j) cm[j] = -m;
        f32x16 O = {};
        u32 va = vadr0;
#pragma unroll 1
        for (int t = 0; t < 64; ++t) {
          f32x16 s = MFMA32(lds_r128(A_KV + t * 2048 + vo), Q, cm);
          u32x4 p0, p1;
#pragma unroll
          for (int j = 0; j < 4; ++j) { p0[j] = pk2(EXP2(s[2 * j]), EXP2(s[2 * j + 1])); p1[j] = pk2(EXP2(s[8 + 2 * j]), EXP2(s[9 + 2 * j])); }
          O = MFMA32(lds_r128(va), p0, O);
          O = MFMA32(lds_r128(va + 512), p1, O);
          va += vstep;
        }
        if (x2) Ob = O; else Oa = O;
      }
    }
    {
      const float ia = __builtin_amdgcn_rcpf(Oa[8]), ib = __builtin_amdgcn_rcpf(Ob[8]);
      const u32x4 oa = {pk2(Oa[0] * ia, Oa[1] * ia), pk2(Oa[2] * ia, Oa[3] * ia), pk2(Oa[4] * ia, Oa[5] * ia), pk2(Oa[6] * ia, Oa[7] * ia)};
      const u32x4 ob = {pk2(Ob[0] * ib, Ob[1] * ib), pk2(Ob[2] * ib, Ob[3] * ib), pk2(Ob[4] * ib, Ob[5] * ib), pk2(Ob[6] * ib, Ob[7] * ib)};
      const u32 o = WS_OF + ((2 * tg + th) * 32 + 4 * hg + 2 * hp) * 1024 + vo;
      bst128(rs, o, noempty4(oa));
      bst128(rs, o + 1024, noempty4(ob));
    }
    STAMP(10);
    {
      u32x4 f[16];
      const u32 src = WS_OF + ((2 * tg + th) * 32 + 16 * hp) * 1024;
      lds_wait<1>(S_FILL, 1);
      STAMP(47);
      __builtin_amdgcn_s_sleep(O_DELAY);
#pragma unroll
      for (int i = 0; i < 16; ++i) f[i] = bld128s(rs, vo, src + i * 1024);
      STAMP(48);
      asm volatile("s_barrier" ::: "memory");
      STAMP(49);
      asm volatile("s_barrier" ::: "memory");
      STAMP(11);
      const u32 wa = T_WO + 16 * hp * 1024 + vo;
      const u32 wk_ = wa + hp * 32768, ws_ = wa + (1 - hp) * 32768;
      u32x4 ring[4];
      auto rdw = [&](int s) { ring[s & 3] = lds_r128(((s & 1) ? ws_ : wk_) + (s >> 1) * 1024); };
#pragma unroll
      for (int s = 0; s < 4; ++s) rdw(s);
      f32x16 ak = {}, as = {};
      settle_s<8>(rs, f, vo, src);
      STAMP(12);
#pragma unroll
      for (int s = 0; s < 32; ++s) {
        if (s == 16) settle_s<8>(rs, f + 8, vo, src + 8192);
        if (s & 1) as = MFMA32(ring[s & 3], f[s >> 1], as); else ak = MFMA32(ring[s & 3], f[s >> 1], ak);
        if (s + 4 < 32) rdw(s + 4);
        SBAR();
      }
      STAMP(13);
      if (wave < 3) {
#pragma unroll
        for (int i = 32 - W_LATE; i < 32; ++i) w[i] = bld128s(rs, vo, WS_W1T + (at * 32 + i) * 1024);
      }
#pragma unroll
      for (int q = 0; q < 4; ++q) lds_wf4(T_XCH + wave * 4096 + q * 1024 + vo, f32x4{as[4 * q], as[4 * q + 1], as[4 * q + 2], as[4 * q + 3]});
      lds_post(S_XCH + wave * 4, 1);
      const int ln = fresh_lane(), r5 = ln & 31, h5 = ln >> 5;
      const u32 xa_ = T_X2 + (32 * th + r5) * 128 + h5 * 8;
      u32x2 xres[4], ga[4];
      u32x4 cb[2];
#pragma unroll
      for (int q = 0; q < 4; ++q) {
        xres[q] = lds_r64(xa_ + (((4 * hp + q) ^ (r5 & 7)) * 16));
        ga[q] = lds_r64(M_GA + (64 * ng + 32 * hp + 8 * q + 4 * h5) * 2);
      }
#pragma unroll
      for (int s = 0; s < 2; ++s) cb[s] = lds_r128(M_CB + (4 * ng + 2 * hp + s) * 32 + h5 * 16);
      lds_wait<0>(S_XCH + (wave ^ 2) * 4, 1);
      u32 xp[8];
      float sm = 0, sq = 0;
#pragma unroll
      for (int q = 0; q < 4; ++q) {
        const f32x4 o4 = lds_rf4(T_XCH + (wave ^ 2) * 4096 + q * 1024 + ln * 16);
        xp[2 * q] = pk2(lo2f(xres[q][0]) + lo2f(ga[q][0]) * (ak[4 * q] + o4[0]), hi2f(xres[q][0]) + hi2f(ga[q][0]) * (ak[4 * q + 1] + o4[1]));
        xp[2 * q + 1] = pk2(lo2f(xres[q][1]) + lo2f(ga[q][1]) * (ak[4 * q + 2] + o4[2]), hi2f(xres[q][1]) + hi2f(ga[q][1]) * (ak[4 * q + 3] + o4[3]));
      }
#pragma unroll
      for (int s = 0; s < 2; ++s) {
        u32x4 y;
#pragma unroll
        for (int d = 0; d < 4; ++d) y[d] = pk2(lo2f(xp[4 * s + d]) * lo2f(cb[s][d]), hi2f(xp[4 * s + d]) * hi2f(cb[s][d]));
        bst128(rs, WS_Y2F + ((2 * tg + th) * 32 + 4 * ng + 2 * hp + s) * 1024 + ln * 16, noempty4(y));
      }
      SBAR();
      STAMP(14);
#pragma unroll
      for (int q = 0; q < 4; ++q) {
        lds_w64(xa_ + (((4 * hp + q) ^ (r5 & 7)) * 16), u32x2{xp[2 * q], xp[2 * q + 1]});
        const float x0 = lo2f(xp[2 * q]), x1 = hi2f(xp[2 * q]), x2_ = lo2f(xp[2 * q + 1]), x3 = hi2f(xp[2 * q + 1]);
        sm += (x0 + x1) + (x2_ + x3);
        sq += (x0 * x0 + x1 * x1) + (x2_ * x2_ + x3 * x3);
      }
      lds_wf(T_STX + hp * 512 + (32 * th + r5) * 8 + h5 * 4, swap32_add(sm, sq));
      if (lds_arrive(S_STARR, ln) == 3) {
        const u32x2 s0 = lds_r64(T_STX + ln * 8), s1 = lds_r64(T_STX + 512 + ln * 8);
        bst64(rs, WS_ST + (tg * 8 + ng) * 512 + ln * 8,
              u32x2{noempty(__float_as_uint(__uint_as_float(s0[0]) + __uint_as_float(s1[0]))),
                    noempty(__float_as_uint(__uint_as_float(s0[1]) + __uint_as_float(s1[1])))});
      }
    }
    if (wave < 3) COLUMN_SUMS();
    STAMP(15);
    WAITV(0);
    if (lds_arrive(S_YL, fresh_lane()) == 3 && fresh_lane() == 0) st4_sc1(ws + WS_YF + (tg * 8 + ng) * 4, tag);
  } else {
    const int j = wave - 4;
#define HP() __builtin_amdgcn_s_setprio(H_PRIO)
#define LP() __builtin_amdgcn_s_setprio(0)
    if (wave < 6) {
      __builtin_amdgcn_s_sleep(KV_DELAY);
      HP();
      u64 m0 = 0, m1 = 0;
      u32x4 fa, fc;
      auto poll = [&]() {
        fa = bld128(rs, WS_KF + vo); fc = bld128(rs, WS_KF + 1024 + vo);
        m0 = __ballot((fa[0] == tag) & (fa[2] == tag)); m1 = __ballot((fc[0] == tag) & (fc[2] == tag));
      };
      auto fetch = [&](int n) {
        const char* src = ws + WS_KV + (8 * ((n + xcd) & 7) + 4 * j) * 2048;
        const u32 dst = lb + A_KV + (8 * n + 4 * j) * 2048;
        glds4_sc1(src, vo, dst); glds4_sc1(src + 4096, vo, dst + 4096);
      };
      _Pragma("unroll 1") for (int spin = 0; spin < SPIN_CAP; ++spin) {
        poll();
        if ((((xcd < 4 ? m0 : m1) >> (16 * (xcd & 3))) & 0xffff) == 0xffff) break;
        __builtin_amdgcn_s_sleep(1);
      }
      STAMP(7);
      lds_wait<1>(S_CONS, 4);
      fetch(0);
      if ((m0 & m1) != ~0ull) {
        _Pragma("unroll 1") for (int spin = 0; spin < SPIN_CAP; ++spin) {
          poll();
          WAITV(0);
          lds_post(S_PROG + j * 4, 1);
          if ((m0 & m1) == ~0ull) break;
          __builtin_amdgcn_s_sleep(1);
        }
      }
      STAMP(29);
#pragma unroll
      for (int n = 1; n < 8; ++n) fetch(n);
      if (wave == 4) {
        const float km = fmaxf(fmaxf(__uint_as_float(fa[1]), __uint_as_float(fa[3])), fmaxf(__uint_as_float(fc[1]), __uint_as_float(fc[3])));
        const float kw = swap32_max(swap16_max(row_max(km)));
        if (lane == 0) lds_wf(S_KM2, kw);
      }
      WAITV(56); lds_post(S_PROG + j * 4, 1);
      WAITV(48); lds_post(S_PROG + j * 4, 2);
      WAITV(0);  lds_post(S_PROG + j * 4, 8);
      STAMP(8);
      LP();
      if (FILL_GATE) lds_wait<4>(S_PREP, 1); else lds_wait<4>(S_PARR, 2);
      HP();
      const u32x4 e4 = {EMPTY, EMPTY, EMPTY, EMPTY};
      const u32 o = (j ? WS_Y2F : WS_OF) + (2 * tg * 32 + 4 * hg) * 1024;
#pragma unroll
      for (int i = 0; i < 8; ++i) bst128s(rs, vo, o + (i >> 2) * 32768 + (i & 3) * 1024, e4);
#pragma unroll
      for (int i = 0; i < 14; ++i) bst128s(rs, vo, WS_HF + ((4 * G + 2 * j + i / 7) * 112 + 7 * cs + i % 7) * 1024, e4);
      if (wave == 5 && lane < 32) bst128(rs, WS_ST + (tg * 8 + ng) * 512 + vo, e4);
      WAITV(0);
      if (lds_arrive(S_FARR, lane) == 1 && lane == 0) st4_sc1(ws + WS_FILL + b * 4, tag);
      STAMP(9);
      LP();
      lds_wait<8>(S_PREP, 1);
      HP();
    } else {
      const int jj = wave - 6;
      wait_kv(RAW_GATE);
      HP();
      STAMP(43);
#pragma unroll
      for (int i3 = 0; i3 < 3; ++i3) {
        const int u = (3 * jj + i3) * 256 + xcd * 32 + idx;
        if (u < 1472) {
          const int R = lane >> 3, cp = lane & 7;
          const char* src; u32 ld;
          if (u < 128)       { src = (const char*)wo + (32 * (u >> 3) + R) * 1024 + (u & 7) * 128 + cp * 16; ld = 1024; }
          else if (u < 1024) { const int e = u - 128, kb = e / 56, ab = e - kb * 56;
                               src = (const char*)(cp < 4 ? wg : wu) + (32 * kb + R) * (FF * 2) + ab * 64 + (cp & 3) * 16; ld = FF * 2; }
          else               { const int e = u - 1024; src = (const char*)wd + (32 * (e >> 3) + R) * 1024 + (e & 7) * 128 + cp * 16; ld = 1024; }
#pragma unroll
          for (int i = 0; i < 4; ++i) glds1_v(src + i * 8 * ld, lb + H_STG + (3 * jj + i3) * 4096 + i * 1024);
        }
      }
      STAMP(31);
      WAITV(0);
      STAMP(32);
      wait_kv(PT_GATE);
      STAMP(7);
#pragma unroll
      for (int i3 = 0; i3 < 3; ++i3) {
        const int u = (3 * jj + i3) * 256 + xcd * 32 + idx;
        const u32 sb = H_STG + (3 * jj + i3) * 4096 + tq_ * 128 + 8 * tp_;
        if (u >= 128 && u < 1024) {
          const int e = u - 128, kb = e / 56, ab = e - kb * 56;
#pragma unroll
          for (int f = 0; f < 4; ++f)
            bst128(rs, WS_W1T + ((2 * ab + (f >> 1)) * 32 + 2 * kb + (f & 1)) * 1024 + vo,
                   lds_tr2(sb + (16 * (f & 1) + 4 * (g >> 1)) * 128 + (g & 1) * 64 + 32 * (f >> 1), 1024));
        } else if (u < 1472) {
          const int e = u < 128 ? u : u - 1024;
          const u32 dst = u < 128 ? WS_WOT : WS_WDT, nks = u < 128 ? 32 : 112;
#pragma unroll
          for (int f = 0; f < 4; ++f)
            bst128(rs, dst + ((2 * (e & 7) + (f >> 1)) * nks + 2 * (e >> 3) + (f & 1)) * 1024 + vo,
                   lds_tr2(sb + (16 * (f & 1) + 4 * (g >> 1)) * 128 + 64 * (f >> 1) + 32 * (g & 1), 1024));
        }
      }
      STAMP(33);
      WAITV(0);
      STAMP(8);
      if (lds_arrive(S_PARR, lane) == 1 && lane == 0) st4_sc1(ws + WS_PREP + b * 4, tag);
      if (wave == 7) {
        _Pragma("unroll 1") for (int spin = 0; spin < SPIN_CAP; ++spin) {
          const u32x4 q = bld128(rs, WS_PREP + vo);
          if (__all((q[0] == tag) & (q[1] == tag) & (q[2] == tag) & (q[3] == tag))) break;
          __builtin_amdgcn_s_sleep(2);
        }
        STAMP(30);
        ORDER_BEHIND_POLL();
        lds_post(S_PREP, 1);
      } else {
        if (idx < 8) {
          lds_wait<2>(S_PARR, 2);
          const int col2 = 32 * (8 * xcd + idx), row2 = (lane >> 2) * 32;
          const char* wsrc = (const char*)wmod + row2 * 6144 + (1024 + col2) * 2 + (lane & 3) * 16;
          float a[8] = {0, 0, 0, 0, 0, 0, 0, 0}, r0, r1;
          u32x4 wm[32], sl[4];
#pragma unroll
          for (int i = 0; i < 32; ++i) wm[i] = *(const u32x4*)(wsrc + i * 6144);
#pragma unroll
          for (int i = 0; i < 4; ++i) sl[i] = lds_r128(M_SILU + row2 * 2 + i * 16);
#pragma unroll
          for (int i = 0; i < 32; ++i) {
            const float si = (i & 1) ? hi2f(sl[i >> 3][(i >> 1) & 3]) : lo2f(sl[i >> 3][(i >> 1) & 3]);
#pragma unroll
            for (int c = 0; c < 4; ++c) { a[2 * c] += si * lo2f(wm[i][c]); a[2 * c + 1] += si * hi2f(wm[i][c]); }
          }
          reduce8q(a, r0, r1);
          const int q = (lane >> 2) & 3, v = 2 * (lane & 3);
          const int l0 = RED8_LANE(v & 3) + q, l1 = RED8_LANE((v & 3) + 1) + q;
          const float e0 = __shfl(r0, l0), e1 = __shfl(r0, l1), o0 = __shfl(r1, l0), o1 = __shfl(r1, l1);
          float m0 = rbf(v < 4 ? e0 : o0), m1 = rbf(v < 4 ? e1 : o1);
          if ((col2 >> 9) == 2) { m0 = rbf(1.f + m0); m1 = rbf(1.f + m1); }
          if (lane < 16) bst64(rs, WS_MOD2 + col2 * 4 + lane * 8, u32x2{tag, pk2(m0, m1)});
        }
        STAMP(30);
        LP();
        lds_wait<8>(S_PREP, 1);
        HP();
      }
      STAMP(9);
      u32x4 xs[4];
#pragma unroll
      for (int i = 0; i < 4; ++i) xs[i] = *(const u32x4*)(x + (64 * tg + 32 * jj + 8 * i + (lane >> 3)) * DM + 64 * ng + 8 * (lane & 7));
#pragma unroll
      for (int i = 0; i < 4; ++i) lds_w128(T_X2 + (32 * jj + 8 * i + (lane >> 3)) * 128 + (((lane & 7) ^ (lane >> 3)) * 16), xs[i]);
    }
    STAMP(10);
    {
      u32x4 wot[16];
      const u32 wi = (j & 1) * 32 + 16 * (j >> 1);
#pragma unroll
      for (int i = 0; i < 16; ++i) wot[i] = bld128s(rs, vo, WS_WOT + (2 * ng * 32 + wi + i) * 1024);
#pragma unroll
      for (int i = 0; i < 32; ++i) w[i] = bld128s(rs, vo, WS_W1T + (at * 32 + i) * 1024);
      WAITV(HW_WAIT);
      STAMP(45);
      lds_arrive(S_HW, lane);
      if (wave == 7) {
        _Pragma("unroll 1") for (int spin = 0; spin < SPIN_CAP; ++spin) {
          const u32x4 q = bld128(rs, WS_FILL + vo);
          if (__all((q[0] == tag) & (q[1] == tag) & (q[2] == tag) & (q[3] == tag))) break;
          __builtin_amdgcn_s_sleep(8);
        }
        ORDER_BEHIND_POLL();
        lds_post(S_FILL, 1);
        if (b == 0 && lane == 0) st4_sc1(ws + WS_EPOCH, tag);
      } else if (wave == 6) {
        _Pragma("unroll 1") for (int spin = 0; spin < SPIN_CAP; ++spin) {
          u32x4 q[8];
#pragma unroll
          for (int i = 0; i < 8; ++i) q[i] = bld128(rs, WS_MOD2 + i * 1024 + vo);
          bool ok = true;
#pragma unroll
          for (int i = 0; i < 8; ++i) ok &= (q[i][0] == tag) & (q[i][2] == tag);
          if (__all(ok)) {
            const u32 kp = (lane >> 2) * 32 + (lane & 1) * 16 + (lane & 2) * 4;
#pragma unroll
            for (int i = 0; i < 2; ++i) {
              lds_w64(M_GA + i * 512 + lane * 8, u32x2{q[i][1], q[i][3]});
              lds_w64(M_SB + i * 512 + kp, u32x2{q[2 + i][1], q[2 + i][3]});
              lds_w64(M_CB + i * 512 + kp, u32x2{q[4 + i][1], q[4 + i][3]});
              lds_w64(M_GB + i * 512 + lane * 8, u32x2{q[6 + i][1], q[6 + i][3]});
            }
            break;
          }
          __builtin_amdgcn_s_sleep(8);
        }
      }
      LP();
      lds_wait<8>(S_AW, AW_GATE);
      STAMP(46);
      LP();
      STAMP(37);
      asm volatile("s_barrier" ::: "memory");
      STAMP(11);
#pragma unroll
      for (int i = 0; i < 16; ++i) lds_w128(T_WO + (wi + i) * 1024 + vo, wot[i]);
      WAITV_BAR(0);
      STAMP(12);
    }
    COLUMN_SUMS();
    STAMP(15);
  }

  const int rot = 2 * (cs & 1);
  {
  const int lane = fresh_lane(), r = lane & 31, hh = lane >> 5;
  const u32 vo = lane * 16;
#define F1_PRIO 0
#define GATE2 1
#define GATE3 1
#define F1_DELAY 0
  if (F1_PRIO == 1 && wave < 3) __builtin_amdgcn_s_setprio(1);
  if (F1_PRIO == 2 && wave > 3) __builtin_amdgcn_s_setprio(1);
  if (wave == 3) __builtin_amdgcn_s_setprio(2);
  const u32 ysrc = WS_Y2F + 4 * G * 32768;
  u32x4 wdr[28];
  const int cF = tg & 1, cL = 1 - cF;
  const u32 wsrc0 = WS_WDT + (2 * ng * 112 + 14 * wave + 7 * cF) * 1024, wsrc1 = WS_WDT + (2 * ng * 112 + 14 * wave + 7 * cL) * 1024;
#define LOAD_WD(I) wdr[I] = bld128s(rs, vo, ((I) < 14 ? wsrc0 + ((I) >> 1) * 1024 : wsrc1 + (((I) >> 1) - 7) * 1024) + ((I) & 1) * 112 * 1024)
  {
    u32x4 f[8];
    const u32 src = ysrc + (((wave < 4 ? 1 : 0) + rot) & 3) * 32768 + 8 * (wave & 3) * 1024;
    if (wave < 4) __builtin_amdgcn_s_sleep(Y_DELAY); else __builtin_amdgcn_s_sleep(Y_DELAY2);
#pragma unroll
    for (int i = 0; i < 8; ++i) f[i] = bld128s(rs, vo, src + i * 1024);
    settle_s<8>(rs, f, vo, src);
#pragma unroll
    for (int i = 0; i < 8; ++i) lds_w128(T_Y + (wave < 4 ? 32768 : 0) + (8 * (wave & 3) + i) * 1024 + vo, f[i]);
    lds_arrive(wave < 4 ? S_Y1 : S_Y0, lane);
  }
  if (wave == 3) {
    u32x4 stp[8];
    const u32 stsrc = WS_ST + 2 * G * 4096, stv = hh * 4096 + r * 16;
#pragma unroll
    for (int i = 0; i < 8; ++i) stp[i] = bld128s(rs, stv, stsrc + i * 512);
    if (__builtin_expect(!filled<8>(stp), 0)) {
      _Pragma("unroll 1") for (int spin = 0; spin < SPIN_CAP; ++spin) {
#pragma unroll
        for (int i = 0; i < 8; ++i) stp[i] = bld128s(rs, stv, stsrc + i * 512);
        if (filled<8>(stp)) break;
        __builtin_amdgcn_s_sleep(1);
      }
    }
    STAMP(16);
    {
      float m0 = 0, m1 = 0, q0 = 0, q1 = 0;
#pragma unroll
      for (int i = 0; i < 8; ++i) {
        m0 += __uint_as_float(stp[i][0]); q0 += __uint_as_float(stp[i][1]); m1 += __uint_as_float(stp[i][2]); q1 += __uint_as_float(stp[i][3]);
      }
      m0 *= 1.f / DM; m1 *= 1.f / DM;
      const float v0 = fmaxf(q0 * (1.f / DM) - m0 * m0, 0.f) + 1e-5f, v1 = fmaxf(q1 * (1.f / DM) - m1 * m1, 0.f) + 1e-5f;
      const float r0 = rsqrtf(v0), r1 = rsqrtf(v1), s0 = v0 * r0, s1 = v1 * r1;
      const float mh0 = rbf(-m0), mh1 = rbf(-m1), sh0 = rbf(s0), sh1 = rbf(s1);
      lds_wait<1>(S_STARR, 4);
      lds_w128(T_BX + (64 * hh + 2 * r) * 16, u32x4{pk2(mh0, -m0 - mh0), pk2(mh0, sh0), pk2(s0 - sh0, sh0), 0u});
      lds_w128(T_BX + (64 * hh + 2 * r) * 16 + 16, u32x4{pk2(mh1, -m1 - mh1), pk2(mh1, sh1), pk2(s1 - sh1, sh1), 0u});
      lds_w64(T_RS + (64 * hh + 2 * r) * 4, u32x2{__float_as_uint(r0), __float_as_uint(r1)});
      lds_post(S_STOK, 1);
    }
    STAMP(18);
    _Pragma("unroll 1") for (int spin = 0; spin < SPIN_CAP; ++spin) {
      if (__all(__builtin_amdgcn_raw_buffer_load_b32(rs, (lane & 15) * 4, WS_YF + 2 * G * 32, SC1) == tag)) break;
      __builtin_amdgcn_s_sleep(1);
    }
    ORDER_BEHIND_POLL();
#pragma unroll
    for (int tt = 2; tt < 4; ++tt) {
      const char* src = ws + ysrc + ((tt + rot) & 3) * 32768;
#pragma unroll
      for (int i = 0; i < 8; ++i) glds4_sc1(src + i * 4096, vo, lb + T_Y + tt * 32768 + i * 4096);
    }
    WAITV(32); lds_post(S_YV, 2);
    WAITV(0);  lds_post(S_YV, 3);
    STAMP(19);
#pragma unroll
    for (int i = 0; i < 28; ++i) LOAD_WD(i);
  } else {
    STAMP(16);
    lds_wait<0>(S_Y0, 4);
    STAMP(17);
    if (F1_DELAY > 0 && wave >= 4 && wave < 7) __builtin_amdgcn_s_sleep(F1_DELAY);
#define F1_D 6
#define EPI_PIECE(P, I)                                                                                \
    if constexpr ((I) >= 0 && (I) < 24) {                                                                      \
      constexpr int e_ = (I) / 3, p_ = (I) % 3;                                                        \
      if (p_ == 0) P[e_] = EXP2(rg * P[e_]);                                                           \
      if (p_ == 1) { P[e_] = __builtin_amdgcn_rcpf(1.f + P[e_]); P[8 + e_] *= rsd; }                   \
      if (p_ == 2) { P[e_] *= P[8 + e_]; if (e_ & 1) hv[e_ >> 1] = noempty(pk2(P[e_ - (e_ & 1)], P[e_])); } \
    }
#define EPI_STORE(TT) bst128s(rs, vo, WS_HF + ((4 * G + (((TT) + rot) & 3)) * 112 + at) * 1024, hv)
#define FFN1_STEP(TT, ACC, P, EXTRA)                                                                   \
    {                                                                                                  \
      if ((TT) == 1) lds_wait<1>(S_Y1, 4);                                                             \
      if ((TT) > 1) lds_wait<1>(S_YV, TT);                                                             \
      const u32 ya = T_Y + (TT) * 32768 + vo;                                                          \
      const u32 ba = T_BX + ((((TT) + rot) & 3) * 32 + r) * 16;                                        \
      u32x4 ring_[F1_D], hv;                                                                           \
      _Pragma("unroll") for (int i = 0; i < F1_D; ++i) ring_[i] = lds_r128(ya + i * 1024);             \
      float rsd = 0, rg = 0;                                                                           \
      if ((TT) > 0) { rsd = lds_rf(T_RS + ((((TT) + 3 + rot) & 3) * 32 + r) * 4); rg = -LOG2E * rsd; } \
      const f32x16 zero = {};                                                                          \
      SBAR();                                                                                          \
      static_for<32>([&](auto I_) {                                                                    \
        constexpr int i = I_;                                                                          \
        ACC = MFMA32(w[i], ring_[i % F1_D], i == 0 ? zero : ACC);                                      \
        if (i + F1_D < 32) ring_[i % F1_D] = lds_r128(ya + (i + F1_D) * 1024);                         \
        else if (i + F1_D == 32 && (TT) > 0) ring_[i % F1_D] = lds_r128(ba);                           \
        if ((TT) > 0) { EPI_PIECE(P, i - 2); if (i == 27) EPI_STORE((TT) - 1); }                       \
        EXTRA;                                                                                         \
        SBAR();                                                                                        \
      });                                                                                              \
      if ((TT) == 0) { lds_wait<1>(S_STOK, 1); ring_[32 % F1_D] = lds_r128(ba); }                      \
      ACC = MFMA32(wX, ring_[32 % F1_D], ACC);                                                         \
      if ((TT) == 1) lds_arrive(S_T1D, lane);                                                          \
      if (GATE2 && (TT) == 3) lds_arrive(S_T2D, lane);                                                 \
      SBAR();                                                                                          \
    }
    f32x16 accA, accB;
#define WD_E 8
#define WD_EARLY(TT) if ((i & 7) == 7 && 4 * (TT) + (i >> 3) < WD_E) LOAD_WD(4 * (TT) + (i >> 3))
    FFN1_STEP(0, accA, accB, WD_EARLY(0));
    STAMP(26);
    FFN1_STEP(1, accB, accA, WD_EARLY(1));
    STAMP(27);
    FFN1_STEP(2, accA, accB, WD_EARLY(2));
    STAMP(28);
    FFN1_STEP(3, accB, accA, if (i < 28 - WD_E) LOAD_WD(WD_E + i));
    STAMP(24);
    {
      const float rsd = lds_rf(T_RS + (((3 + rot) & 3) * 32 + r) * 4), rg = -LOG2E * rsd;
      u32x4 hv;
      static_for<24>([&](auto I_) { constexpr int i = I_; EPI_PIECE(accB, (i % 8) * 3 + i / 8); });
      EPI_STORE(3);
      if (GATE3) lds_arrive(S_T3D, lane);
    }
  }
  __builtin_amdgcn_s_setprio(0);
  STAMP(20);
  {
    u32x4 hb[14], hl[7];
    const u32 h00 = WS_HF + (2 * tg * 112 + 14 * wave + 7 * cF) * 1024, h01 = WS_HF + (2 * tg * 112 + 14 * wave + 7 * cL) * 1024;
    const u32 h10 = h00 + 112 * 1024, h11 = h01 + 112 * 1024;
#pragma unroll
    for (int i = 0; i < 7; ++i) hb[i] = bld128s(rs, vo, h00 + i * 1024);
#pragma unroll
    for (int i = 0; i < 7; ++i) hl[i] = bld128s(rs, vo, h10 + i * 1024);
    lds_wait<2>(S_T2D, 7);
#pragma unroll
    for (int i = 0; i < 7; ++i) hb[7 + i] = bld128s(rs, vo, h01 + i * 1024);
    f32x16 c0 = {}, c1 = {}, d0 = {}, d1 = {};
    settle_s<7>(rs, hb, vo, h00);
    STAMP(21);
#pragma unroll
    for (int i = 0; i < 7; ++i) { c0 = MFMA32(wdr[2 * i], hb[i], c0); c1 = MFMA32(wdr[2 * i + 1], hb[i], c1); }
    settle_s<7>(rs, hl, vo, h10);
#pragma unroll
    for (int i = 0; i < 7; ++i) { d0 = MFMA32(wdr[2 * i], hl[i], d0); d1 = MFMA32(wdr[2 * i + 1], hl[i], d1); }
    SBAR();
    settle_s<7>(rs, hb + 7, vo, h01);
#pragma unroll
    for (int i = 7; i < 14; ++i) { c0 = MFMA32(wdr[2 * i], hb[i], c0); c1 = MFMA32(wdr[2 * i + 1], hb[i], c1); }
    SBAR();
    STAMP(22);
    if (GATE3) lds_wait<2>(S_T3D, 7);
    __builtin_amdgcn_s_sleep(H_DELAY);
#pragma unroll
    for (int i = 0; i < 7; ++i) hl[i] = bld128s(rs, vo, h11 + i * 1024);
    SBAR();
    const int ln = fresh_lane(), r5 = ln & 31, h5 = ln >> 5;
    const u32 ro = T_RED + wave * 8192 + h5 * 512;
#define DUMP(OFF, C0, C1)                                                                              \
    _Pragma("unroll") for (int q = 0; q < 4; ++q) {                                                    \
      lds_wf4(ro + (OFF) + q * 1024 + ((r5 ^ (2 * q + h5)) * 16), f32x4{C0[4 * q], C0[4 * q + 1], C0[4 * q + 2], C0[4 * q + 3]}); \
      lds_wf4(ro + (OFF) + 4096 + q * 1024 + ((r5 ^ (8 + 2 * q + h5)) * 16), f32x4{C1[4 * q], C1[4 * q + 1], C1[4 * q + 2], C1[4 * q + 3]}); \
    }
#define OUT_STORE(P_, A_, B_) *(u32x2*)(P_) = u32x2{A_, B_}
#define REDUCE(P)                                                                                      \
    {                                                                                                  \
      const int cq = ln & 15, tk = 4 * wave + (ln >> 4);                                               \
      const u32 ra = T_RED + (P) * 65536 + (cq >> 1) * 1024 + (cq & 1) * 512 + ((tk ^ cq) * 16);       \
      f32x4 a = {0, 0, 0, 0};                                                                          \
      _Pragma("unroll") for (int v = 0; v < 8; ++v) a += lds_rf4(ra + v * 8192);                       \
      const u32x2 x2 = lds_r64(T_X2 + (32 * (P) + tk) * 128 + (((cq >> 1) ^ (tk & 7)) * 16) + (cq & 1) * 8); \
      const u32x2 gb = lds_r64(M_GB + (64 * ng + 4 * cq) * 2);                                         \
      OUT_STORE(out + (64 * tg + 32 * (P) + tk) * DM + 64 * ng + 4 * cq,                               \
          pk2(lo2f(x2[0]) + lo2f(gb[0]) * a[0], hi2f(x2[0]) + hi2f(gb[0]) * a[1]),                     \
          pk2(lo2f(x2[1]) + lo2f(gb[1]) * a[2], hi2f(x2[1]) + hi2f(gb[1]) * a[3]));                    \
    }
    lds_wait<1>(S_T1D, 7);
    DUMP(0, c0, c1);
    BAR();
    STAMP(23);
    REDUCE(0);
    settle_s<7>(rs, hl, vo, h11);
#pragma unroll
    for (int i = 0; i < 7; ++i) { d0 = MFMA32(wdr[14 + 2 * i], hl[i], d0); d1 = MFMA32(wdr[15 + 2 * i], hl[i], d1); }
    DUMP(65536, d0, d1);
    BAR();
    REDUCE(1);
  }
  }
  STAMPE(25);
}

extern "C" void kernel_launch(void* const* d_in, const int* in_sizes, int n_in, void* d_out, int out_size, void* d_ws, size_t ws_size,
                              hipStream_t stream) {
  (void)in_sizes; (void)n_in; (void)out_size; (void)ws_size;
  (void)hipFuncSetAttribute((const void*)fused_block, hipFuncAttributeMaxDynamicSharedMemorySize, LDS_BYTES);
  fused_block<<<256, 512, LDS_BYTES, stream>>>((const u16*)d_in[0], (const u16*)d_in[1], (const u16*)d_in[2], (const u16*)d_in[3],
                                               (const u16*)d_in[4], (const u16*)d_in[6], (const u16*)d_in[7], (const u16*)d_in[8],
                                               (const u16*)d_in[9], (const u16*)d_in[10], (char*)d_ws, (u16*)d_out);
}
```

```cpp
#include <hip/hip_runtime.h>
#include <hip/hip_bf16.h>
#include <stdint.h>

typedef unsigned short u16;
typedef unsigned int u32;
typedef unsigned long long u64;
typedef __bf16 bf16x8 __attribute__((ext_vector_type(8)));
typedef __bf16 bf16x2 __attribute__((ext_vector_type(2)));
typedef short s16x4 __attribute__((ext_vector_type(4)));
typedef float f32x16 __attribute__((ext_vector_type(16)));
typedef float f32x4 __attribute__((ext_vector_type(4)));
typedef float f32x2 __attribute__((ext_vector_type(2)));
typedef u32 u32x4 __attribute__((ext_vector_type(4)));
typedef u32 u32x2 __attribute__((ext_vector_type(2)));
#define LDSP __attribute__((address_space(3)))
#define GLBP __attribute__((address_space(1)))

constexpr int SEQ = 2048, DM = 512, FF = 1792;
constexpr float LOG2E = 1.4426950408889634f;

constexpr u32 WS_EPOCH = 0;
constexpr u32 WS_KF    = 1024;
constexpr u32 WS_PREP  = 3072;
constexpr u32 WS_MODG  = 4096;
constexpr u32 WS_YF    = 13312;
constexpr u32 WS_MOD2  = 16384;
constexpr u32 WS_FILL  = 12288;
constexpr u32 WS_ST    = 24576;
constexpr u32 WS_MODP  = WS_ST + 131072;
constexpr u32 WS_KV    = WS_MODP + 32768;
constexpr u32 WS_WOT   = WS_KV + 131072;
constexpr u32 WS_W1T   = WS_WOT + 524288;
constexpr u32 WS_WDT   = WS_W1T + 3584 * 1024;
constexpr u32 WS_OF    = WS_WDT + 1792 * 1024;
constexpr u32 WS_Y2F   = WS_OF + 2097152;
constexpr u32 WS_HF    = WS_Y2F + 2097152;
constexpr u32 WS_END   = WS_HF + 64 * 112 * 1024;

__device__ __forceinline__ float bf2f(u32 v) { return __uint_as_float(v << 16); }
__device__ __forceinline__ float lo2f(u32 w) { return __uint_as_float(w << 16); }
__device__ __forceinline__ float hi2f(u32 w) { return __uint_as_float(w & 0xffff0000u); }
__device__ __forceinline__ u32 pk2(float a, float b) {
  f32x2 v = {a, b};
  return __builtin_bit_cast(u32, __builtin_convertvector(v, bf16x2));
}
__device__ __forceinline__ float rbf(float a) { return lo2f(pk2(a, a)); }
__device__ __forceinline__ bf16x8 asbf(u32x4 v) { return __builtin_bit_cast(bf16x8, v); }
#define MFMA32(a, b, c) __builtin_amdgcn_mfma_f32_32x32x16_bf16(asbf(a), asbf(b), c, 0, 0, 0)
#define MFMA16(a, b, c) __builtin_amdgcn_mfma_f32_16x16x32_bf16(asbf(a), asbf(b), c, 0, 0, 0)
#define SBAR() __builtin_amdgcn_sched_barrier(0)
#define PIN(x) asm volatile("" : "+v"(x))
#define BAR() asm volatile("s_waitcnt lgkmcnt(0)\n\ts_barrier" ::: "memory")
#define WAITV_(N) asm volatile("s_waitcnt vmcnt(" #N ")" ::: "memory")
#define WAITV(N) WAITV_(N)
#define WAITV_BAR(N) asm volatile("s_waitcnt vmcnt(" #N ") lgkmcnt(0)\n\ts_barrier" ::: "memory")
#define RLX_AGENT __ATOMIC_RELAXED, __HIP_MEMORY_SCOPE_AGENT
constexpr int SC1 = 16;
#define SPIN_LOG 16
constexpr int SPIN_CAP = 1 << SPIN_LOG;

template <int CTRL>
__device__ __forceinline__ float dpp(float x) {
  return __int_as_float(__builtin_amdgcn_update_dpp(0, __float_as_int(x), CTRL, 0xf, 0xf, true));
}
__device__ __forceinline__ float row_sum(float x) {
  x += dpp<0x128>(x); x += dpp<0x124>(x); x += dpp<0x122>(x); x += dpp<0x121>(x); return x;
}
__device__ __forceinline__ float row_max(float x) {
  x = fmaxf(x, dpp<0x128>(x)); x = fmaxf(x, dpp<0x124>(x)); x = fmaxf(x, dpp<0x122>(x)); x = fmaxf(x, dpp<0x121>(x)); return x;
}
__device__ __forceinline__ float swap32_add(float a, float b) {
  auto r = __builtin_amdgcn_permlane32_swap(__float_as_uint(a), __float_as_uint(b), false, false);
  return __uint_as_float(r[0]) + __uint_as_float(r[1]);
}
__device__ __forceinline__ float swap16_add(float a, float b) {
  auto r = __builtin_amdgcn_permlane16_swap(__float_as_uint(a), __float_as_uint(b), false, false);
  return __uint_as_float(r[0]) + __uint_as_float(r[1]);
}
__device__ __forceinline__ float swap32_sum(float x) { return swap32_add(x, x); }
__device__ __forceinline__ float swap16_sum(float x) { return swap16_add(x, x); }
__device__ __forceinline__ float swap32_max(float x) {
  auto r = __builtin_amdgcn_permlane32_swap(__float_as_uint(x), __float_as_uint(x), false, false);
  return fmaxf(__uint_as_float(r[0]), __uint_as_float(r[1]));
}
__device__ __forceinline__ float swap16_max(float x) {
  auto r = __builtin_amdgcn_permlane16_swap(__float_as_uint(x), __float_as_uint(x), false, false);
  return fmaxf(__uint_as_float(r[0]), __uint_as_float(r[1]));
}
__device__ __forceinline__ void reduce8(const float (&v)[8], float& r0, float& r1) {
  const float a0 = swap32_add(v[0], v[1]), a1 = swap32_add(v[2], v[3]), a2 = swap32_add(v[4], v[5]), a3 = swap32_add(v[6], v[7]);
  r0 = row_sum(swap16_add(a0, a1));
  r1 = row_sum(swap16_add(a2, a3));
}
__device__ __forceinline__ void reduce8q(const float (&v)[8], float& r0, float& r1) {
  const float a0 = swap32_add(v[0], v[1]), a1 = swap32_add(v[2], v[3]), a2 = swap32_add(v[4], v[5]), a3 = swap32_add(v[6], v[7]);
  r0 = swap16_add(a0, a1); r1 = swap16_add(a2, a3);
  r0 += dpp<0x128>(r0); r0 += dpp<0x124>(r0); r1 += dpp<0x128>(r1); r1 += dpp<0x124>(r1);
}
__device__ __forceinline__ float rdlane(float x, int l) { return __int_as_float(__builtin_amdgcn_readlane(__float_as_int(x), l)); }
#define RED8_LANE(i) (16 * ((((i) & 1) << 1) | (((i) >> 1) & 1)))

template <int V> struct ic { static constexpr int value = V; constexpr operator int() const { return V; } };
template <int N, int I = 0, class F>
__device__ __forceinline__ void static_for(F&& f) { if constexpr (I < N) { f(ic<I>{}); static_for<N, I + 1>(f); } }

extern __shared__ __attribute__((aligned(16))) char smem[];
__device__ __forceinline__ u32x4 lds_r128(u32 off) { return *(const LDSP u32x4*)((LDSP char*)smem + off); }
__device__ __forceinline__ void lds_w128(u32 off, u32x4 v) { *(LDSP u32x4*)((LDSP char*)smem + off) = v; }
__device__ __forceinline__ f32x4 lds_rf4(u32 off) { return *(const LDSP f32x4*)((LDSP char*)smem + off); }
__device__ __forceinline__ void lds_wf4(u32 off, f32x4 v) { *(LDSP f32x4*)((LDSP char*)smem + off) = v; }
__device__ __forceinline__ float lds_rf(u32 off) { return *(const LDSP float*)((LDSP char*)smem + off); }
__device__ __forceinline__ void lds_wf(u32 off, float v) { *(LDSP float*)((LDSP char*)smem + off) = v; }
__device__ __forceinline__ u32x2 lds_r64(u32 off) { return *(const LDSP u32x2*)((LDSP char*)smem + off); }
__device__ __forceinline__ void lds_w64(u32 off, u32x2 v) { *(LDSP u32x2*)((LDSP char*)smem + off) = v; }
__device__ __forceinline__ void lds_w32(u32 off, u32 v) { *(LDSP u32*)((LDSP char*)smem + off) = v; }
__device__ __forceinline__ u32 lds_r32(u32 off) { return *(const LDSP u32*)((LDSP char*)smem + off); }
__device__ __forceinline__ u32x4 lds_r128a(u32 addr) { return *(const LDSP u32x4*)(uintptr_t)addr; }
__device__ __forceinline__ u32 lds_base() { return (u32)(uintptr_t)(LDSP char*)smem; }
typedef short v4i16_t __attribute__((ext_vector_type(4)));
__device__ __forceinline__ u32x2 lds_tr(u32 off) {
  return __builtin_bit_cast(u32x2, __builtin_amdgcn_ds_read_tr16_b64_v4i16((LDSP v4i16_t*)((LDSP char*)smem + off)));
}
__device__ __forceinline__ u32x4 lds_tr2(u32 off, u32 step) {
  const u32x2 a = lds_tr(off), c = lds_tr(off + step);
  return u32x4{a[0], a[1], c[0], c[1]};
}

#define GLDS_ASM(NAME, BODY)                                                                             \
  __device__ __forceinline__ void NAME(const void* g, u32 voff, u32 lds) {                               \
    u32 sv;                                                                                              \
    asm volatile("s_mov_b32 %0, m0\n\ts_mov_b32 m0, %3\n\ts_nop 0\n\t" BODY "s_mov_b32 m0, %0"           \
                 : "=&s"(sv) : "v"(voff), "s"(g), "s"(lds) : "memory");                                  \
  }
GLDS_ASM(glds1, "global_load_lds_dwordx4 %1, %2\n\t")
GLDS_ASM(glds1_sc1, "global_load_lds_dwordx4 %1, %2 sc1\n\t")
GLDS_ASM(glds2_sc1, "global_load_lds_dwordx4 %1, %2 sc1\n\tglobal_load_lds_dwordx4 %1, %2 offset:1024 sc1\n\t")
GLDS_ASM(glds4_sc1, "global_load_lds_dwordx4 %1, %2 sc1\n\tglobal_load_lds_dwordx4 %1, %2 offset:1024 sc1\n\t"
                    "global_load_lds_dwordx4 %1, %2 offset:2048 sc1\n\tglobal_load_lds_dwordx4 %1, %2 offset:3072 sc1\n\t")
#undef GLDS_ASM

__device__ __forceinline__ u32 ld4_sc1(const void* p) { return __hip_atomic_load((GLBP u32*)p, RLX_AGENT); }
__device__ __forceinline__ void st4_sc1(void* p, u32 v) { __hip_atomic_store((GLBP u32*)p, v, RLX_AGENT); }
__device__ __forceinline__ void st8_sc1(void* p, u32 lo, u32 hi) {
  __hip_atomic_store((GLBP u64*)p, ((u64)hi << 32) | lo, RLX_AGENT);
}
typedef __amdgpu_buffer_rsrc_t rsrc_t;
__device__ __forceinline__ u32x4 bld128(rsrc_t rs, u32 off) { return __builtin_amdgcn_raw_buffer_load_b128(rs, off, 0, SC1); }
__device__ __forceinline__ u32x2 bld64(rsrc_t rs, u32 off) { return __builtin_amdgcn_raw_buffer_load_b64(rs, off, 0, SC1); }
__device__ __forceinline__ void bst128(rsrc_t rs, u32 off, u32x4 v) { __builtin_amdgcn_raw_buffer_store_b128(v, rs, off, 0, SC1); }
__device__ __forceinline__ void bst64(rsrc_t rs, u32 off, u32x2 v) { __builtin_amdgcn_raw_buffer_store_b64(v, rs, off, 0, SC1); }
__device__ __forceinline__ u32x4 bld128s(rsrc_t rs, u32 voff, u32 soff) { return __builtin_amdgcn_raw_buffer_load_b128(rs, voff, soff, SC1); }
__device__ __forceinline__ void bst128s(rsrc_t rs, u32 voff, u32 soff, u32x4 v) { __builtin_amdgcn_raw_buffer_store_b128(v, rs, voff, soff, SC1); }
#define ORDER_BEHIND_POLL() __builtin_amdgcn_fence(__ATOMIC_ACQUIRE, "wavefront")

extern __shared__ __attribute__((aligned(16))) u32 smem32[];
__device__ __forceinline__ u32 lds_peek(u32 off) {
  return __hip_atomic_load((LDSP u32*)smem32 + off / 4, __ATOMIC_RELAXED, __HIP_MEMORY_SCOPE_WORKGROUP);
}
__device__ __forceinline__ void lds_post(u32 off, u32 v) {
  asm volatile("" ::: "memory");
  __hip_atomic_store((LDSP u32*)smem32 + off / 4, v, __ATOMIC_RELAXED, __HIP_MEMORY_SCOPE_WORKGROUP);
}
__device__ __forceinline__ u32 lds_arrive(u32 off, int lane) {
  asm volatile("" ::: "memory");
  u32 old = 0;
  if (lane == 0) old = __hip_atomic_fetch_add((LDSP u32*)smem32 + off / 4, 1u, __ATOMIC_RELAXED, __HIP_MEMORY_SCOPE_WORKGROUP);
  old = __builtin_amdgcn_readfirstlane(old);
  asm volatile("" ::: "memory");
  return old;
}
template <int SLEEP>
__device__ __forceinline__ void lds_wait(u32 off, u32 v) {
  _Pragma("unroll 1") for (int spin = 0; spin < SPIN_CAP; ++spin) {
    if ((u32)__builtin_amdgcn_readfirstlane(lds_peek(off)) >= v) break;
    __builtin_amdgcn_s_sleep(SLEEP);
  }
  asm volatile("" ::: "memory");
}
__device__ __forceinline__ int fresh_lane() {
  int l; asm volatile("v_mbcnt_lo_u32_b32 %0, -1, 0\n\tv_mbcnt_hi_u32_b32 %0, -1, %0" : "=v"(l)); return l;
}
constexpr u32 EMPTY = 0xffffffffu;
__device__ __forceinline__ u32 umax(u32 a, u32 b) { return a > b ? a : b; }
__device__ __forceinline__ u32 noempty(u32 v) { return v < EMPTY ? v : EMPTY - 1; }
__device__ __forceinline__ u32x4 noempty4(u32x4 v) { return u32x4{noempty(v[0]), noempty(v[1]), noempty(v[2]), noempty(v[3])}; }
template <int N>
__device__ __forceinline__ bool filled(const u32x4* f) {
  u32 m = 0, m2 = 0;
#pragma unroll
  for (int i = 0; i < N; ++i) { m = umax(umax(m, f[i][0]), f[i][1]); m2 = umax(umax(m2, f[i][2]), f[i][3]); }
  return __all(umax(m, m2) != EMPTY);
}
template <int N>
__device__ __forceinline__ void settle(rsrc_t rs, u32x4* f, u32 off) {
  if (__builtin_expect(!filled<N>(f), 0)) {
    _Pragma("unroll 1") for (int spin = 0; spin < SPIN_CAP; ++spin) {
#pragma unroll
      for (int i = 0; i < N; ++i) f[i] = bld128(rs, off + i * 1024);
      if (filled<N>(f)) break;
      __builtin_amdgcn_s_sleep(1);
    }
  }
}

template <int N>
__device__ __forceinline__ void settle_s(rsrc_t rs, u32x4* f, u32 voff, u32 soff) {
  if (__builtin_expect(!filled<N>(f), 0)) {
    _Pragma("unroll 1") for (int spin = 0; spin < SPIN_CAP; ++spin) {
#pragma unroll
      for (int i = 0; i < N; ++i) f[i] = bld128s(rs, voff, soff + i * 1024);
      if (filled<N>(f)) break;
      __builtin_amdgcn_s_sleep(1);
    }
  }
}

#define STAMP_DECL()
#define STAMP(I)
#define STAMPE(I)
#define KV_DELAY 12
#define O_DELAY 11
#define Y_DELAY 10
#define Y_DELAY2 17
#define H_DELAY 1
#define H_DELAY2 0

constexpr u32 P_WQ = 0, P_H = 65536, P_WKV = 131072, P_M1 = 147456, P_PART = 149504;
constexpr u32 A_KV = 0, H_STG = 131072;
constexpr u32 T_WO = 65536, T_Y = 0, T_RED = 0, T_XCH = 131072, T_X2 = 147456,
              T_STX = 155648, T_US = 157696, T_BX = T_XCH, T_RS = T_XCH + 2048;
constexpr u32 L_ONES = 155648, L_SYNC = 159616, L_MODI = 159744, LDS_BYTES = 163840;
constexpr u32 S_PROG = L_SYNC, S_CONS = L_SYNC + 16, S_PARR = L_SYNC + 20, S_PREP = L_SYNC + 24, S_STARR = L_SYNC + 28,
              S_YV = L_SYNC + 32, S_STOK = L_SYNC + 36, S_KM2 = L_SYNC + 64, S_T1D = L_SYNC + 44, S_XCH = L_SYNC + 68;
constexpr u32 S_T2D = L_SYNC + 48, S_T3D = L_SYNC + 52, S_Y0 = L_SYNC + 56, S_YL = L_SYNC + 84, S_Y1 = L_SYNC + 88, S_T0D = L_SYNC + 92,
              S_T2C = L_SYNC + 96, S_WS = L_SYNC + 100, S_WRD = L_SYNC + 104, S_D0 = L_SYNC + 108, S_M2 = L_SYNC + 116;
constexpr u32 S_FARR = L_SYNC + 8, S_FILL = L_SYNC + 12, S_HW = L_SYNC + 40, S_AW = L_SYNC + 60;
constexpr u32 M_GA = L_MODI, M_SB = L_MODI + 1024, M_CB = L_MODI + 2048, M_GB = L_MODI + 3072, M_SILU = L_MODI;
constexpr float QSCALE = 0.25f * LOG2E;
constexpr float BOUND2 = 64.f * 64.f;
#define EXP2(x) __builtin_amdgcn_exp2f(x)

__device__ __forceinline__ u32x4 ald128(const void* g, u32 voff) {
  u32x4 v; asm volatile("global_load_dwordx4 %0, %1, %2" : "=v"(v) : "v"(voff), "s"(g) : "memory"); return v;
}
__device__ __forceinline__ u32x2 ald64(const void* g, u32 voff) {
  u32x2 v; asm volatile("global_load_dwordx2 %0, %1, %2" : "=v"(v) : "v"(voff), "s"(g) : "memory"); return v;
}
__device__ __forceinline__ u32 ald16(const void* g, u32 voff) {
  u32 v; asm volatile("global_load_ushort %0, %1, %2" : "=v"(v) : "v"(voff), "s"(g) : "memory"); return v;
}
__device__ __forceinline__ u32 ald32(const void* p) {
  u32 v; asm volatile("global_load_dword %0, %1, off" : "=v"(v) : "v"(p) : "memory"); return v;
}
__device__ __forceinline__ u32 ald32_sc1(const void* g, u32 voff) {
  u32 v; asm volatile("global_load_dword %0, %1, %2 sc1" : "=v"(v) : "v"(voff), "s"(g) : "memory"); return v;
}
__device__ __forceinline__ void glds1_v(const void* p, u32 lds) {
  u32 sv;
  asm volatile("s_mov_b32 %0, m0\n\ts_mov_b32 m0, %2\n\ts_nop 0\n\tglobal_load_lds_dwordx4 %1, off\n\ts_mov_b32 m0, %0"
               : "=&s"(sv) : "v"(p), "s"(lds) : "memory");
}

__device__ __forceinline__ void pf_line(const void* g, u32 voff, u32 lds) {
  u32 sv;
  asm volatile("s_mov_b32 %0, m0\n\ts_mov_b32 m0, %3\n\ts_nop 0\n\tglobal_load_lds_dword %1, %2 sc1\n\ts_mov_b32 m0, %0"
               : "=&s"(sv) : "v"(voff), "s"(g), "s"(lds) : "memory");
}
#define W1_AT 24
#define HW_WAIT 8
#define AW_GATE 4
#define RAW_GATE 2
#define PT_GATE 8
#define FILL_GATE 0
#define KV_PROG 1
#define M2_LP 0
#define M2_ALL 1
#define M2_GATE 0
#define MOD2_EARLY 0
#define FILL_LATE 0
#define W_LATE 4
#define H_PRIO 2
#define EXP_(Sc, I) Sc[I] = EXP2(Sc[I])
#define CVT_(P, J, Sc, I) P[J] = pk2(Sc[I], Sc[(I) + 1]); PIN(P[J]); SBAR()
#define HALF(Sc, Sn, Kn, Qn, Oc, V0, V1, CIN, GAP)                                                     \
  do {                                                                                                 \
    u32x4 p0_, p1_;                                                                                    \
    EXP_(Sc, 0); EXP_(Sc, 1); EXP_(Sc, 2); EXP_(Sc, 3); SBAR();                                        \
    CVT_(p0_, 0, Sc, 0); CVT_(p0_, 1, Sc, 2);                                                          \
    Sn = MFMA32(Kn, Qn, CIN); SBAR();                                                                  \
    EXP_(Sc, 4); EXP_(Sc, 5); EXP_(Sc, 6); EXP_(Sc, 7); SBAR();                                        \
    CVT_(p0_, 2, Sc, 4); CVT_(p0_, 3, Sc, 6);                                                          \
    EXP_(Sc, 8); EXP_(Sc, 9); SBAR();                                                                  \
    Oc = MFMA32(V0, p0_, Oc); SBAR();                                                                  \
    EXP_(Sc, 10); EXP_(Sc, 11); SBAR();                                                                \
    CVT_(p1_, 0, Sc, 8); CVT_(p1_, 1, Sc, 10);                                                         \
    EXP_(Sc, 12); EXP_(Sc, 13); EXP_(Sc, 14); EXP_(Sc, 15); SBAR();                                    \
    CVT_(p1_, 2, Sc, 12); CVT_(p1_, 3, Sc, 14);                                                        \
    GAP; SBAR();                                                                                       \
    Oc = MFMA32(V1, p1_, Oc); SBAR();                                                                  \
  } while (0)
#define LGKM0() __builtin_amdgcn_s_waitcnt(0xC07F)
#define TWO_TILES(X0, X1, X2, X3)                                                                      \
  do {                                                                                                 \
    u32x4 W0, W1;                                                                                      \
    HALF(Sa, Sb, Kc, Qb, Oa, V0, V1, zero, W0 = lds_r128a(vadr + 2048); W1 = lds_r128a(vadr + 2560); Kc = lds_r128a(ka + 4096)); \
    X0; SBAR();                                                                                        \
    HALF(Sb, Sa, Kn, Qa, Ob, V0, V1, zero, LGKM0(); SBAR(); vadr += vstep2; PIN(vadr));                \
    X1; SBAR();                                                                                        \
    HALF(Sa, Sb, Kn, Qb, Oa, W0, W1, zero, V0 = lds_r128a(vadr); V1 = lds_r128a(vadr + 512); Kn = lds_r128a(ka + 6144)); \
    X2; SBAR();                                                                                        \
    HALF(Sb, Sa, Kc, Qa, Ob, W0, W1, zero, LGKM0(); SBAR(); ka += 4096; PIN(ka));                      \
    X3; SBAR();                                                                                        \
  } while (0)
__device__ __forceinline__ u32 wait_kv(u32 n) {
  u32 have = 0;
  _Pragma("unroll 1") for (int spin = 0; spin < SPIN_CAP; ++spin) {
    asm volatile("" ::: "memory");
    const u32x2 p = lds_r64(S_PROG);
    have = __builtin_amdgcn_readfirstlane(p[0] < p[1] ? p[0] : p[1]);
    if (have >= n) break;
    __builtin_amdgcn_s_sleep(1);
  }
  asm volatile("" ::: "memory");
  return have;
}
#define CHAIN32(ACC, ADDR, STRIDE, DEPTH, EXTRA)                                                       \
  {                                                                                                    \
    u32x4 ring_[DEPTH];                                                                                \
    _Pragma("unroll") for (int i = 0; i < DEPTH; ++i) ring_[i] = lds_r128((ADDR) + i * (STRIDE));      \
    SBAR();                                                                                            \
    _Pragma("unroll") for (int i = 0; i < 32; ++i) {                                                   \
      ACC = MFMA32(w[i], ring_[i % DEPTH], ACC);                                                       \
      if (i + DEPTH < 32) ring_[i % DEPTH] = lds_r128((ADDR) + (i + DEPTH) * (STRIDE));                \
      EXTRA;                                                                                           \
      SBAR();                                                                                          \
    }                                                                                                  \
  }
#define COLUMN_SUMS()                                                                                  \
  {                                                                                                    \
    f32x16 us = {};                                                                                    \
    const int ln_ = fresh_lane();                                                                      \
    const u32 ma = ((ln_ & 1) ? M_SB : M_CB) + (ln_ >> 5) * 16;                                        \
    CHAIN32(us, ma, 32, 8, );                                                                          \
    const u32 ub = T_US + (at - 7 * cs) * 256;                                                         \
    if ((ln_ & 31) < 2) {                                                                              \
      _Pragma("unroll") for (int q = 0; q < 4; ++q)                                                    \
        lds_wf4(ub + (ln_ & 1) * 128 + (ln_ >> 5) * 16 + q * 32, f32x4{us[4 * q], us[4 * q + 1], us[4 * q + 2], us[4 * q + 3]}); \
    }                                                                                                  \
    const float u_ = lds_rf(ub + (ln_ & 31) * 4), s_ = lds_rf(ub + 128 + (ln_ & 31) * 4);              \
    const float uh = rbf(u_), sh = rbf(s_);                                                            \
    wX = u32x4{pk2(uh, uh), pk2(u_ - uh, sh), pk2(sh, s_ - sh), 0u};                                   \
    if (ln_ >= 32) wX = u32x4{0u, 0u, 0u, 0u};                                                         \
  }

__global__ __launch_bounds__(512, 2) void fused_block(const u16* __restrict__ x, const u16* __restrict__ wq, const u16* __restrict__ wk,
                                                      const u16* __restrict__ wv, const u16* __restrict__ wo, const u16* __restrict__ wg,
                                                      const u16* __restrict__ wu, const u16* __restrict__ wd, const u16* __restrict__ cvec,
                                                      const u16* __restrict__ wmod, char* __restrict__ ws, u16* __restrict__ out) {
  const int tid = threadIdx.x, lane = tid & 63, b = blockIdx.x;
  const int wave = __builtin_amdgcn_readfirstlane(tid >> 6);
  const int r = lane & 31, hh = lane >> 5, g = lane >> 4, li = lane & 15;
  const int xcd = b & 7, idx = b >> 3;
  const int tg = xcd * 4 + (idx >> 3), hg = idx & 7;
  const int ng = hg, G = 4 * (xcd >> 1) + (idx >> 3), cs = 8 * (xcd & 1) + (idx & 7);
  const u32 lb = lds_base(), vo = lane * 16;
  const rsrc_t rs = __builtin_amdgcn_make_buffer_rsrc((void*)ws, 0, WS_END, 0x00020000);
  const int tq_ = li >> 2, tp_ = lane & 3;
  STAMP_DECL();
#define PROBE_AT(N)

#define MODP 2
#ifndef M_DELAY
#define M_DELAY 4
#endif
  constexpr int RPT = 4 / MODP;
  const bool prodA = idx < 4 * MODP;
  const int colA = 32 * (4 * xcd + (idx & 3));
  const int rowA = prodA ? (idx >> 2) * (512 / MODP) + (tid >> 2) * RPT : 0;
  u32 ev = ald32_sc1(ws + WS_EPOCH, 0);
  u32 cv = ald16(cvec, tid * 2);
  u32 cr = RPT == 1 ? ald16(cvec, rowA * 2) : ald32_sc1(cvec, rowA * 2);
  u32x4 wA[RPT];
#pragma unroll
  for (int i = 0; i < RPT; ++i) wA[i] = ald128(wmod, prodA ? (rowA + i) * 6144 + colA * 2 + (lane & 3) * 16 : 0);
#define P_BAR 1
  if (P_BAR && prodA) asm volatile("s_barrier" ::: "memory");
  const int kq = hg >> 1;
  auto row_of = [&](int i) { return i < 2 ? 16 * kq + 2 * wave + i : (16 * kq + 14 + 6 * wave + i) & 63; };
  u32x4 xr[8];
#pragma unroll
  for (int i = 0; i < 8; ++i) xr[i] = ald128(x + (64 * tg + row_of(i)) * DM, vo);
  lds_w32(L_ONES + tid * 4, 0x3f803f80u);
  if (tid < 256) lds_w32(L_ONES + 2048 + tid * 4, 0x3f803f80u);
  if (tid < 32) lds_w32(L_SYNC + tid * 4, 0u);
  if constexpr (RPT == 1) asm volatile("s_waitcnt vmcnt(8)" : "+v"(ev), "+v"(cv), "+v"(cr), "+v"(wA[0])::"memory");
  else asm volatile("s_waitcnt vmcnt(8)" : "+v"(ev), "+v"(cv), "+v"(cr), "+v"(wA[0]), "+v"(wA[RPT - 1])::"memory");
  const u32 tag = __builtin_amdgcn_readfirstlane(ev) + 1;
  STAMP(50);
  {
    const float f = bf2f(cv), sl = rbf(f * __builtin_amdgcn_rcpf(1.f + EXP2(-LOG2E * f)));
    *(LDSP u16*)((LDSP char*)smem + M_SILU + tid * 2) = (u16)(__float_as_uint(sl) >> 16);
    if (prodA) {
      float a[8] = {0, 0, 0, 0, 0, 0, 0, 0}, r0, r1;
      const int sub = ((g & 1) << 1) | (g >> 1);
#pragma unroll
      for (int i = 0; i < RPT; ++i) {
        const float fr = i == 0 ? lo2f(cr) : hi2f(cr), si = rbf(fr * __builtin_amdgcn_rcpf(1.f + EXP2(-LOG2E * fr)));
#pragma unroll
        for (int j = 0; j < 4; ++j) { a[2 * j] += si * lo2f(wA[i][j]); a[2 * j + 1] += si * hi2f(wA[i][j]); }
      }
      reduce8q(a, r0, r1);
      if (li < 4) { lds_wf(P_PART + (wave * 32 + li * 8 + sub) * 4, r0); lds_wf(P_PART + (wave * 32 + li * 8 + 4 + sub) * 4, r1); }
      BAR();
      if (wave == 0) {
        const int jl = lane & 31;
        float s = 0;
#pragma unroll
        for (int w = 0; w < 8; ++w) s += lds_rf(P_PART + (w * 32 + jl) * 4);
        if (lane < 32) st8_sc1(ws + WS_MODP + ((idx >> 2) * 1024 + colA + jl) * 8, tag, __float_as_uint(s));
      }
    }
  }
  STAMP(1);
  float xh[8][8];
  asm volatile("s_waitcnt vmcnt(0)"
               : "+v"(xr[0]), "+v"(xr[1]), "+v"(xr[2]), "+v"(xr[3]), "+v"(xr[4]), "+v"(xr[5]), "+v"(xr[6]), "+v"(xr[7])::"memory");
  if (wave == 7) {
    const u16* wkv = (hg & 1) ? wv : wk;
#pragma unroll
    for (int i = 0; i < 16; ++i) glds1(wkv + i * 512, vo, lb + P_WKV + i * 1024);
  }
  STAMP(2);
  {
    float s[8], r0, r1;
#pragma unroll
    for (int i = 0; i < 8; ++i) {
#pragma unroll
      for (int j = 0; j < 4; ++j) { xh[i][2 * j] = lo2f(xr[i][j]); xh[i][2 * j + 1] = hi2f(xr[i][j]); }
      s[i] = ((xh[i][0] + xh[i][1]) + (xh[i][2] + xh[i][3])) + ((xh[i][4] + xh[i][5]) + (xh[i][6] + xh[i][7]));
    }
    reduce8(s, r0, r1);
#pragma unroll
    for (int i = 0; i < 8; ++i) {
      const float m = rdlane(i < 4 ? r0 : r1, RED8_LANE(i)) * (1.f / DM);
      float q = 0;
#pragma unroll
      for (int j = 0; j < 8; ++j) { xh[i][j] -= m; q += xh[i][j] * xh[i][j]; }
      s[i] = q;
    }
    reduce8(s, r0, r1);
    r0 = rsqrtf(r0 * (1.f / DM) + 1e-5f); r1 = rsqrtf(r1 * (1.f / DM) + 1e-5f);
#pragma unroll
    for (int i = 0; i < 8; ++i) {
      const float rstd = rdlane(i < 4 ? r0 : r1, RED8_LANE(i));
#pragma unroll
      for (int j = 0; j < 8; ++j) xh[i][j] *= rstd;
    }
  }
  STAMP(3);
  if (wave < 4) {
    const u32 vq = (lane >> 3) * 1024 + hg * 128 + (((lane & 7) ^ (((lane >> 4) & 1) << 2)) * 16);
#pragma unroll
    for (int i = 0; i < 16; ++i) glds1(wq + (128 * wave + 8 * i) * DM, vq, lb + P_WQ + (128 * wave + 8 * i) * 128);
  }
  u32x4 wf[16];
  if (wave == 5 || wave == 6) {
    const u32 msrc = WS_MODP + (wave - 5) * 4096;
    if (M_DELAY) __builtin_amdgcn_s_sleep(M_DELAY);
    _Pragma("unroll 1") for (int spin = 0; spin < SPIN_CAP; ++spin) {
      u32x4 q[4][MODP];
#pragma unroll
      for (int i = 0; i < 4; ++i)
#pragma unroll
        for (int p = 0; p < MODP; ++p) q[i][p] = bld128s(rs, vo, msrc + p * 8192 + i * 1024);
      bool ok = true;
#pragma unroll
      for (int i = 0; i < 4; ++i) {
        float s0 = 0, s1 = 0;
#pragma unroll
        for (int p = 0; p < MODP; ++p) {
          ok &= (q[i][p][0] == tag) & (q[i][p][2] == tag);
          s0 += __uint_as_float(q[i][p][1]); s1 += __uint_as_float(q[i][p][3]);
        }
        if (wave == 6) { s0 = 1.f + rbf(s0); s1 = 1.f + rbf(s1); }
        lds_w32(P_M1 + (wave - 5) * 1024 + i * 256 + lane * 4, pk2(s0, s1));
      }
      if (__all(ok)) break;
      __builtin_amdgcn_s_sleep(1);
    }
  } else if (wave == 7) {
    WAITV(0);
    const u32 wa = P_WKV + (8 * g + tq_) * 32 + 8 * tp_;
#pragma unroll
    for (int t = 0; t < 16; ++t) wf[t] = lds_tr2(wa + t * 1024, 128);
  }
  BAR();
  STAMP(4);
  PROBE_AT(3);
  float kn2 = 0;
  {
    const u32x4 sa = lds_r128(P_M1 + vo), ca = lds_r128(P_M1 + 1024 + vo);
    float sf[8], cf[8];
#pragma unroll
    for (int j = 0; j < 4; ++j) { sf[2 * j] = lo2f(sa[j]); sf[2 * j + 1] = hi2f(sa[j]); cf[2 * j] = lo2f(ca[j]); cf[2 * j + 1] = hi2f(ca[j]); }
    auto put_row = [&](int i) {
      const int rho = row_of(i), rr = rho & 31;
      u32x4 hv;
#pragma unroll
      for (int j = 0; j < 4; ++j) hv[j] = pk2(xh[i][2 * j] * cf[2 * j] + sf[2 * j], xh[i][2 * j + 1] * cf[2 * j + 1] + sf[2 * j + 1]);
      lds_w128(P_H + (rho >> 5) * 32768 + (lane >> 1) * 1024 + (lane & 1) * 512 + ((rr ^ li) * 16), hv);
    };
    put_row(0); put_row(1);
    BAR();
    if (wave == 7) {
      __builtin_amdgcn_s_setprio(3);
      const int q = kq, m = hg & 1;
      const int rr = (q & 1) * 16 + li;
      f32x4 d0 = {0, 0, 0, 0}, d1 = {0, 0, 0, 0};
      const u32 ha = P_H + (q >> 1) * 32768 + (g >> 1) * 1024 + (g & 1) * 512;
      u32x4 hf[16];
#pragma unroll
      for (int t = 0; t < 16; ++t) hf[t] = lds_r128(ha + t * 2048 + ((rr ^ ((4 * t + g) & 15)) * 16));
      SBAR();
      if (m == 0) {
#pragma unroll
        for (int t = 0; t < 16; t += 2) { d0 = MFMA16(wf[t], hf[t], d0); d1 = MFMA16(wf[t + 1], hf[t + 1], d1); }
      } else {
#pragma unroll
        for (int t = 0; t < 16; t += 2) { d0 = MFMA16(hf[t], wf[t], d0); d1 = MFMA16(hf[t + 1], wf[t + 1], d1); }
      }
      d0 += d1;
      const u32x2 pkd = {pk2(d0[0], d0[1]), pk2(d0[2], d0[3])};
      const u32 dst = WS_KV + (2 * tg + (q >> 1)) * 2048 + (g >> 1) * 8;
      if (m == 0) {
        bst64(rs, dst + ((g & 1) * 32 + rr) * 16, pkd);
        const float k0 = lo2f(pkd[0]), k1 = hi2f(pkd[0]), k2 = lo2f(pkd[1]), k3 = hi2f(pkd[1]);
        kn2 = row_max(swap32_sum(swap16_sum(k0 * k0 + k1 * k1 + k2 * k2 + k3 * k3)));
      } else {
        bst64(rs, dst + 1024 + ((q & 1) * 32 + (g & 1) * 16 + li) * 16, pkd);
      }
      __builtin_amdgcn_s_setprio(0);
      STAMP(35);
    }
#pragma unroll
    for (int i = 2; i < 8; ++i) put_row(i);
  }
  if (wave < 4) WAITV(0);
  BAR();
  STAMP(5);
  if (wave == 7) {
    WAITV(0);
    if (lane == 0) st8_sc1(ws + WS_KF + (tg * 8 + hg) * 8, tag, __float_as_uint(kn2));
    STAMP(36);
  }

  u32x4 w[32], wX;
  const int at = 7 * cs + (wave < 4 ? 4 + wave : wave - 4);
  if (wave < 4) {
    const int th = wave & 1, hp = wave >> 1;
    u32x4 Qa, Qb; float qn2;
    {
      f32x16 qa = {};
      const u32 a0 = P_WQ + (8 * (g >> 1) + tq_) * 128 + (((4 * hp + 2 * (g & 1) + (tp_ >> 1)) ^ ((tq_ >> 1) << 2)) * 16) + 8 * (tp_ & 1);
      u32x4 ra[4], rb[4];
      auto rdq = [&](int ks) {
        ra[ks & 3] = lds_tr2(a0 + ks * 2048, 512);
        rb[ks & 3] = lds_r128(P_H + th * 32768 + ks * 1024 + hh * 512 + ((r ^ ((2 * ks + hh) & 15)) * 16));
      };
#pragma unroll
      for (int ks = 0; ks < 4; ++ks) rdq(ks);
      SBAR();
#pragma unroll
      for (int ks = 0; ks < 32; ++ks) {
        qa = MFMA32(ra[ks & 3], rb[ks & 3], qa);
        if (ks + 4 < 32) rdq(ks + 4);
        SBAR();
      }
      lds_arrive(S_CONS, lane);
#pragma unroll
      for (int j = 0; j < 4; ++j) {
        Qa[j] = pk2(qa[2 * j] * QSCALE, qa[2 * j + 1] * QSCALE); Qb[j] = pk2(qa[8 + 2 * j] * QSCALE, qa[9 + 2 * j] * QSCALE);
      }
      float na = 0, nb = 0;
#pragma unroll
      for (int j = 0; j < 4; ++j) {
        na += lo2f(Qa[j]) * lo2f(Qa[j]) + hi2f(Qa[j]) * hi2f(Qa[j]);
        nb += lo2f(Qb[j]) * lo2f(Qb[j]) + hi2f(Qb[j]) * hi2f(Qb[j]);
      }
      qn2 = fmaxf(swap32_sum(na), swap32_sum(nb));
    }
    STAMP(6);
    f32x16 Oa = {}, Ob = {};
    const u32 vadr0 = (r < 16) ? (A_KV + 1024 + hh * 256 + r * 16) : (L_ONES + ((r >= 20 && r < 28) ? 64 : 0));
    const u32 vstep = (r < 16) ? 2048u : 0u;
    {
      f32x16 Sa, Sb;
      const f32x16 zero = {};
      u32 vadr = lb + vadr0, ka = lb + A_KV + vo;
      const u32 vstep2 = 2 * vstep;
      wait_kv(1);
      __builtin_amdgcn_s_setprio(1);
      STAMP(7);
      u32x4 Kc = lds_r128a(ka);
      Sa = MFMA32(Kc, Qa, zero);
      u32x4 V0 = lds_r128a(vadr), V1 = lds_r128a(vadr + 512), Kn = lds_r128a(ka + 2048);
      LGKM0();
      int t = 0;
      {
        u32 have = 1;
#pragma unroll 1
        for (int seg = 0; seg < 7; ++seg) {
          const int tend = 6 + 8 * seg;
#pragma unroll 1
          for (; t < tend; t += 2) TWO_TILES(, , , );
          if (seg == 0) STAMP(38); else if (seg == 1) STAMP(40); else if (seg == 2) STAMP(42);
          if (have < (u32)seg + 2) have = wait_kv(seg + 2);
          if (seg == 0) STAMP(39); else if (seg == 1) STAMP(41);
        }
      }
#pragma unroll 1
      for (; t < 56; t += 2) TWO_TILES(, , , );
      STAMP(8);
      lds_wait<1>(S_PREP, 1);
      STAMP(34);
#define W1_REQ(I) if ((I) < 32 - W_LATE) { w[I] = bld128s(rs, vo + ((I) & 3) * 1024, w1src + ((I) >> 2) * 4096); w[(I) + 1] = bld128s(rs, vo + (((I) + 1) & 3) * 1024, w1src + ((I) >> 2) * 4096); }
#define W1_POS 1
      if (W1_POS == 0 && wave < 3) {
        const u32 w1src = WS_W1T + at * 32768;
#pragma unroll
        for (int u = 0; u < 4; ++u) {
          TWO_TILES(W1_REQ(8 * u), W1_REQ(8 * u + 2), W1_REQ(8 * u + 4), W1_REQ(8 * u + 6));
          t += 2;
        }
      }
      lds_arrive(S_AW, lane);
      STAMP(44);
#pragma unroll 1
      for (; t < 64; t += 2) TWO_TILES(, , , );
    }
    __builtin_amdgcn_s_setprio(0);
    STAMP(9);
    PROBE_AT(4);
    if (__any(!(qn2 * lds_rf(S_KM2) <= BOUND2))) {
#pragma unroll 1
      for (int x2 = 0; x2 < 2; ++x2) {
        const u32x4 Q = x2 ? Qb : Qa;
        float m = -3.0e38f;
        const f32x16 zero = {};
#pragma unroll 1
        for (int t = 0; t < 64; ++t) {
          const f32x16 s = MFMA32(lds_r128(A_KV + t * 2048 + vo), Q, zero);
#pragma unroll
          for (int j = 0; j < 16; ++j) m = fmaxf(m, s[j]);
        }
        m = swap32_max(m);
        f32x16 cm;
#pragma unroll
        for (int j = 0; j < 16; ++j) cm[j] = -m;
        f32x16 O = {};
        u32 va = vadr0;
#pragma unroll 1
        for (int t = 0; t < 64; ++t) {
          f32x16 s = MFMA32(lds_r128(A_KV + t * 2048 + vo), Q, cm);
          u32x4 p0, p1;
#pragma unroll
          for (int j = 0; j < 4; ++j) { p0[j] = pk2(EXP2(s[2 * j]), EXP2(s[2 * j + 1])); p1[j] = pk2(EXP2(s[8 + 2 * j]), EXP2(s[9 + 2 * j])); }
          O = MFMA32(lds_r128(va), p0, O);
          O = MFMA32(lds_r128(va + 512), p1, O);
          va += vstep;
        }
        if (x2) Ob = O; else Oa = O;
      }
    }
    {
      const float ia = __builtin_amdgcn_rcpf(Oa[8]), ib = __builtin_amdgcn_rcpf(Ob[8]);
      const u32x4 oa = {pk2(Oa[0] * ia, Oa[1] * ia), pk2(Oa[2] * ia, Oa[3] * ia), pk2(Oa[4] * ia, Oa[5] * ia), pk2(Oa[6] * ia, Oa[7] * ia)};
      const u32x4 ob = {pk2(Ob[0] * ib, Ob[1] * ib), pk2(Ob[2] * ib, Ob[3] * ib), pk2(Ob[4] * ib, Ob[5] * ib), pk2(Ob[6] * ib, Ob[7] * ib)};
      const u32 o = WS_OF + ((2 * tg + th) * 32 + 4 * hg + 2 * hp) * 1024 + vo;
      bst128(rs, o, noempty4(oa));
      bst128(rs, o + 1024, noempty4(ob));
    }
    STAMP(10);
    {
      u32x4 f[16];
      const u32 src = WS_OF + ((2 * tg + th) * 32 + 16 * hp) * 1024;
#define W1_ALL() if (wave < 3) { const u32 w1src = WS_W1T + at * 32768; \
        _Pragma("unroll") for (int i = 0; i < 32 - W_LATE; ++i) w[i] = bld128s(rs, vo + (i & 3) * 1024, w1src + (i >> 2) * 4096); }
      if (W1_POS == 2) W1_ALL();
#define BAR1_EARLY 1
#define W1_N 8
      if (BAR1_EARLY) asm volatile("s_barrier" ::: "memory");
      lds_wait<1>(S_FILL, 1);
      STAMP(47);
      __builtin_amdgcn_s_sleep(O_DELAY);
#pragma unroll
      for (int i = 0; i < 16; ++i) f[i] = bld128s(rs, vo, src + i * 1024);
      if (W1_POS == 1) {
        const u32 w1src = WS_W1T + (wave < 3 ? at : 0) * 32768, vw = wave < 3 ? vo : 0x40000000u;
#pragma unroll
        for (int i = 0; i < W1_N; ++i) w[i] = bld128s(rs, vw + (i & 3) * 1024, w1src + (i >> 2) * 4096);
      }
      STAMP(48);
      if (!BAR1_EARLY) asm volatile("s_barrier" ::: "memory");
      STAMP(49);
      asm volatile("s_barrier" ::: "memory");
      STAMP(11);
      const u32 wa = T_WO + 16 * hp * 1024 + vo;
      const u32 wk_ = wa + hp * 32768, ws_ = wa + (1 - hp) * 32768;
      u32x4 ring[4];
      auto rdw = [&](int s) { ring[s & 3] = lds_r128(((s & 1) ? ws_ : wk_) + (s >> 1) * 1024); };
#pragma unroll
      for (int s = 0; s < 4; ++s) rdw(s);
      f32x16 ak = {}, as = {};
      const u32 w1src3 = WS_W1T + (wave < 3 ? at : 0) * 32768, vw3 = wave < 3 ? vo : 0x40000000u;
      settle_s<8>(rs, f, vo, src);
      STAMP(12);
#pragma unroll
      for (int s = 0; s < 32; ++s) {
        if (s == 16) settle_s<8>(rs, f + 8, vo, src + 8192);
        if (s & 1) as = MFMA32(ring[s & 3], f[s >> 1], as); else ak = MFMA32(ring[s & 3], f[s >> 1], ak);
        if (s + 4 < 32) rdw(s + 4);
        if (W1_POS == 3 && s < 32 - W_LATE) w[s] = bld128s(rs, vw3 + (s & 3) * 1024, w1src3 + (s >> 2) * 4096);
        if (W1_POS == 1 && W1_N + s < 32 - W_LATE) w[W1_N + s] = bld128s(rs, vw3 + ((W1_N + s) & 3) * 1024, w1src3 + ((W1_N + s) >> 2) * 4096);
        SBAR();
      }
      STAMP(13);
      if (wave < 3) {
#pragma unroll
        for (int i = 32 - W_LATE; i < 32; ++i) w[i] = bld128s(rs, vo, WS_W1T + (at * 32 + i) * 1024);
      }
#pragma unroll
      for (int q = 0; q < 4; ++q) lds_wf4(T_XCH + wave * 4096 + q * 1024 + vo, f32x4{as[4 * q], as[4 * q + 1], as[4 * q + 2], as[4 * q + 3]});
      lds_post(S_XCH + wave * 4, 1);
      const int ln = fresh_lane(), r5 = ln & 31, h5 = ln >> 5;
      const u32 xa_ = T_X2 + (32 * th + r5) * 128 + h5 * 8;
      u32x2 xres[4], ga[4];
      u32x4 cb[2];
#pragma unroll
      for (int q = 0; q < 4; ++q) {
        xres[q] = lds_r64(xa_ + (((4 * hp + q) ^ (r5 & 7)) * 16));
        ga[q] = lds_r64(M_GA + (64 * ng + 32 * hp + 8 * q + 4 * h5) * 2);
      }
#pragma unroll
      for (int s = 0; s < 2; ++s) cb[s] = lds_r128(M_CB + (4 * ng + 2 * hp + s) * 32 + h5 * 16);
      lds_wait<0>(S_XCH + (wave ^ 2) * 4, 1);
      u32 xp[8];
      float sm = 0, sq = 0;
#pragma unroll
      for (int q = 0; q < 4; ++q) {
        const f32x4 o4 = lds_rf4(T_XCH + (wave ^ 2) * 4096 + q * 1024 + ln * 16);
        xp[2 * q] = pk2(lo2f(xres[q][0]) + lo2f(ga[q][0]) * (ak[4 * q] + o4[0]), hi2f(xres[q][0]) + hi2f(ga[q][0]) * (ak[4 * q + 1] + o4[1]));
        xp[2 * q + 1] = pk2(lo2f(xres[q][1]) + lo2f(ga[q][1]) * (ak[4 * q + 2] + o4[2]), hi2f(xres[q][1]) + hi2f(ga[q][1]) * (ak[4 * q + 3] + o4[3]));
      }
#pragma unroll
      for (int s = 0; s < 2; ++s) {
        u32x4 y;
#pragma unroll
        for (int d = 0; d < 4; ++d) y[d] = pk2(lo2f(xp[4 * s + d]) * lo2f(cb[s][d]), hi2f(xp[4 * s + d]) * hi2f(cb[s][d]));
        bst128(rs, WS_Y2F + ((2 * tg + th) * 32 + 4 * ng + 2 * hp + s) * 1024 + ln * 16, noempty4(y));
      }
      SBAR();
      STAMP(14);
#pragma unroll
      for (int q = 0; q < 4; ++q) {
        lds_w64(xa_ + (((4 * hp + q) ^ (r5 & 7)) * 16), u32x2{xp[2 * q], xp[2 * q + 1]});
        const float x0 = lo2f(xp[2 * q]), x1 = hi2f(xp[2 * q]), x2_ = lo2f(xp[2 * q + 1]), x3 = hi2f(xp[2 * q + 1]);
        sm += (x0 + x1) + (x2_ + x3);
        sq += (x0 * x0 + x1 * x1) + (x2_ * x2_ + x3 * x3);
      }
      lds_wf(T_STX + hp * 512 + (32 * th + r5) * 8 + h5 * 4, swap32_add(sm, sq));
      if (lds_arrive(S_STARR, ln) == 3) {
        const u32x2 s0 = lds_r64(T_STX + ln * 8), s1 = lds_r64(T_STX + 512 + ln * 8);
        bst64(rs, WS_ST + (tg * 8 + ng) * 512 + ln * 8,
              u32x2{noempty(__float_as_uint(__uint_as_float(s0[0]) + __uint_as_float(s1[0]))),
                    noempty(__float_as_uint(__uint_as_float(s0[1]) + __uint_as_float(s1[1])))});
      }
    }
    if (wave < 3) COLUMN_SUMS();
    STAMP(15);
    WAITV(0);
    if (lds_arrive(S_YL, fresh_lane()) == 3 && fresh_lane() == 0) st4_sc1(ws + WS_YF + (tg * 8 + ng) * 4, tag);
  } else {
    const int j = wave - 4;
#define HP() __builtin_amdgcn_s_setprio(H_PRIO)
#define LP() __builtin_amdgcn_s_setprio(0)
    if (wave < 6) {
      __builtin_amdgcn_s_sleep(KV_DELAY);
      HP();
      u64 m0 = 0, m1 = 0;
      u32x4 fa, fc;
      auto poll = [&]() {
        fa = bld128(rs, WS_KF + vo); fc = bld128(rs, WS_KF + 1024 + vo);
        m0 = __ballot((fa[0] == tag) & (fa[2] == tag)); m1 = __ballot((fc[0] == tag) & (fc[2] == tag));
      };
      auto fetch = [&](int n) {
        const char* src = ws + WS_KV + (8 * ((n + xcd) & 7) + 4 * j) * 2048;
        const u32 dst = lb + A_KV + (8 * n + 4 * j) * 2048;
        glds4_sc1(src, vo, dst); glds4_sc1(src + 4096, vo, dst + 4096);
      };
      _Pragma("unroll 1") for (int spin = 0; spin < SPIN_CAP; ++spin) {
        poll();
        if ((((xcd < 4 ? m0 : m1) >> (16 * (xcd & 3))) & 0xffff) == 0xffff) break;
        __builtin_amdgcn_s_sleep(1);
      }
      STAMP(7);
      lds_wait<1>(S_CONS, 4);
      fetch(0);
      u32 nf = 1, posted = 0;
      _Pragma("unroll 1") for (int spin = 0; spin < SPIN_CAP; ++spin) {
        poll();
        WAITV(0);
        posted = nf;
        lds_post(S_PROG + j * 4, nf);
        const u32 n0 = nf;
        _Pragma("unroll 1") while (nf < 8) {
          const int gq = (nf + xcd) & 7;
          if ((((gq < 4 ? m0 : m1) >> (16 * (gq & 3))) & 0xffff) != 0xffff) break;
          fetch(nf); ++nf;
        }
        if (nf == 8) break;
        if (nf > n0 + 1) {
          switch (nf - n0) {
            case 2: WAITV(8); break;
            case 3: WAITV(16); break;
            case 4: WAITV(24); break;
            case 5: WAITV(32); break;
            default: WAITV(40); break;
          }
          posted = n0 + 1;
          lds_post(S_PROG + j * 4, n0 + 1);
        }
        __builtin_amdgcn_s_sleep(1);
      }
      STAMP(29);
      if (wave == 4) {
        const float km = fmaxf(fmaxf(__uint_as_float(fa[1]), __uint_as_float(fa[3])), fmaxf(__uint_as_float(fc[1]), __uint_as_float(fc[3])));
        const float kw = swap32_max(swap16_max(row_max(km)));
        if (lane == 0) lds_wf(S_KM2, kw);
      }
      WAITV(48); if (posted < 2) lds_post(S_PROG + j * 4, 2);
      WAITV(40); if (posted < 3) lds_post(S_PROG + j * 4, 3);
      WAITV(24); if (posted < 5) lds_post(S_PROG + j * 4, 5);
      WAITV(0);  lds_post(S_PROG + j * 4, 8);
      STAMP(8);
      LP();
#define FILL_A()                                                                                       \
      {                                                                                                \
        const u32x4 e4 = {EMPTY, EMPTY, EMPTY, EMPTY};                                                 \
        const u32 o = (j ? WS_Y2F : WS_OF) + (2 * tg * 32 + 4 * hg) * 1024;                            \
        _Pragma("unroll") for (int i = 0; i < 8; ++i) bst128s(rs, vo, o + (i >> 2) * 32768 + (i & 3) * 1024, e4); \
        if (wave == 5 && lane < 32) bst128(rs, WS_ST + (tg * 8 + ng) * 512 + vo, e4);                  \
      }
#define FILL_B()                                                                                       \
      {                                                                                                \
        const u32x4 e4 = {EMPTY, EMPTY, EMPTY, EMPTY};                                                 \
        _Pragma("unroll") for (int i = 0; i < 14; ++i) bst128s(rs, vo, WS_HF + ((4 * G + 2 * j + i / 7) * 112 + 7 * cs + i % 7) * 1024, e4); \
      }
#define FILL_END() WAITV(0); if (lds_arrive(S_FARR, lane) == 1 && lane == 0) st4_sc1(ws + WS_FILL + b * 4, tag)
      if (FILL_LATE != 1) {
        if (FILL_GATE) lds_wait<4>(S_PREP, 1); else lds_wait<4>(S_PARR, 2);
        if (M2_GATE && idx < 8) lds_wait<4>(S_M2, 1);
        HP();
        FILL_A();
        if (FILL_LATE == 0) { FILL_B(); FILL_END(); }
      }
      STAMP(9);
      LP();
      lds_wait<8>(S_PREP, 1);
      HP();
    } else {
      const int jj = wave - 6;
      wait_kv(RAW_GATE);
      HP();
      STAMP(43);
#pragma unroll
      for (int i3 = 0; i3 < 3; ++i3) {
        const int u = (3 * jj + i3) * 256 + xcd * 32 + idx;
        if (u < 1472) {
          const int R = lane >> 3, cp = lane & 7;
          const char* src; u32 ld;
          if (u < 128)       { src = (const char*)wo + (32 * (u >> 3) + R) * 1024 + (u & 7) * 128 + cp * 16; ld = 1024; }
          else if (u < 1024) { const int e = u - 128, kb = e / 56, ab = e - kb * 56;
                               src = (const char*)(cp < 4 ? wg : wu) + (32 * kb + R) * (FF * 2) + ab * 64 + (cp & 3) * 16; ld = FF * 2; }
          else               { const int e = u - 1024; src = (const char*)wd + (32 * (e >> 3) + R) * 1024 + (e & 7) * 128 + cp * 16; ld = 1024; }
#pragma unroll
          for (int i = 0; i < 4; ++i) glds1_v(src + i * 8 * ld, lb + H_STG + (3 * jj + i3) * 4096 + i * 1024);
        }
      }
      STAMP(31);
      WAITV(0);
      STAMP(32);
      wait_kv(PT_GATE);
      STAMP(7);
#pragma unroll
      for (int i3 = 0; i3 < 3; ++i3) {
        const int u = (3 * jj + i3) * 256 + xcd * 32 + idx;
        const u32 sb = H_STG + (3 * jj + i3) * 4096 + tq_ * 128 + 8 * tp_;
        if (u >= 128 && u < 1024) {
          const int e = u - 128, kb = e / 56, ab = e - kb * 56;
#pragma unroll
          for (int f = 0; f < 4; ++f)
            bst128(rs, WS_W1T + ((2 * ab + (f >> 1)) * 32 + 2 * kb + (f & 1)) * 1024 + vo,
                   lds_tr2(sb + (16 * (f & 1) + 4 * (g >> 1)) * 128 + (g & 1) * 64 + 32 * (f >> 1), 1024));
        } else if (u < 1472) {
          const int e = u < 128 ? u : u - 1024;
          const u32 dst = u < 128 ? WS_WOT : WS_WDT, nks = u < 128 ? 32 : 112;
#pragma unroll
          for (int f = 0; f < 4; ++f)
            bst128(rs, dst + ((2 * (e & 7) + (f >> 1)) * nks + 2 * (e >> 3) + (f & 1)) * 1024 + vo,
                   lds_tr2(sb + (16 * (f & 1) + 4 * (g >> 1)) * 128 + 64 * (f >> 1) + 32 * (g & 1), 1024));
        }
      }
      STAMP(33);
      const int col2 = 32 * (8 * xcd + idx), row2 = (lane >> 2) * 32;
      u32x4 wm[32];
      if (MOD2_EARLY && wave == 6 && idx < 8) {
        const char* wsrc = (const char*)wmod + row2 * 6144 + (1024 + col2) * 2 + (lane & 3) * 16;
#pragma unroll
        for (int i = 0; i < 32; ++i) wm[i] = *(const u32x4*)(wsrc + i * 6144);
        WAITV(32);
      } else WAITV(0);
      STAMP(8);
      if (lds_arrive(S_PARR, lane) == 1 && lane == 0) st4_sc1(ws + WS_PREP + b * 4, tag);
      if (wave == 7) {
        _Pragma("unroll 1") for (int spin = 0; spin < SPIN_CAP; ++spin) {
          const u32x4 q = bld128(rs, WS_PREP + vo);
          if (__all((q[0] == tag) & (q[1] == tag) & (q[2] == tag) & (q[3] == tag))) break;
          __builtin_amdgcn_s_sleep(2);
        }
        STAMP(30);
        ORDER_BEHIND_POLL();
        lds_post(S_PREP, 1);
      } else {
        {
          lds_wait<2>(S_PARR, 2);
          const int c8 = 8 * (32 * xcd + idx);
          const char* wsrc = (const char*)wmod + lane * (8 * 6144) + (1024 + c8) * 2;
          u32x4 wr[8];
#pragma unroll
          for (int i = 0; i < 8; ++i) wr[i] = *(const u32x4*)(wsrc + i * 6144);
          const u32x4 sl = lds_r128(M_SILU + lane * 16);
          float a[8] = {0, 0, 0, 0, 0, 0, 0, 0}, r0, r1;
#pragma unroll
          for (int i = 0; i < 8; ++i) {
            const float si = (i & 1) ? hi2f(sl[i >> 1]) : lo2f(sl[i >> 1]);
#pragma unroll
            for (int c = 0; c < 4; ++c) { a[2 * c] += si * lo2f(wr[i][c]); a[2 * c + 1] += si * hi2f(wr[i][c]); }
          }
          reduce8(a, r0, r1);
          const int v = 2 * (lane & 3), l0 = RED8_LANE(v & 3), l1 = RED8_LANE((v & 3) + 1);
          const float e0 = __shfl(r0, l0), e1 = __shfl(r0, l1), o0 = __shfl(r1, l0), o1 = __shfl(r1, l1);
          float m0 = rbf(v < 4 ? e0 : o0), m1 = rbf(v < 4 ? e1 : o1);
          if ((c8 >> 9) == 2) { m0 = rbf(1.f + m0); m1 = rbf(1.f + m1); }
          if (lane < 4) bst64(rs, WS_MOD2 + c8 * 4 + lane * 8, u32x2{tag, pk2(m0, m1)});
        }
        STAMP(30);
        LP();
        lds_wait<8>(S_PREP, 1);
        HP();
      }
      STAMP(9);
      u32x4 xs[4];
#pragma unroll
      for (int i = 0; i < 4; ++i) xs[i] = *(const u32x4*)(x + (64 * tg + 32 * jj + 8 * i + (lane >> 3)) * DM + 64 * ng + 8 * (lane & 7));
#pragma unroll
      for (int i = 0; i < 4; ++i) lds_w128(T_X2 + (32 * jj + 8 * i + (lane >> 3)) * 128 + (((lane & 7) ^ (lane >> 3)) * 16), xs[i]);
    }
    STAMP(10);
    {
      u32x4 wot[16];
      const u32 wi = (j & 1) * 32 + 16 * (j >> 1);
#pragma unroll
      for (int i = 0; i < 16; ++i) wot[i] = bld128s(rs, vo, WS_WOT + (2 * ng * 32 + wi + i) * 1024);
#pragma unroll
      for (int i = 0; i < 32; ++i) w[i] = bld128s(rs, vo, WS_W1T + (at * 32 + i) * 1024);
      if (FILL_LATE && wave < 6) {
        if (FILL_LATE == 1) FILL_A();
        FILL_B();
        FILL_END();
      }
      WAITV(HW_WAIT);
      STAMP(45);
      lds_arrive(S_HW, lane);
#define PF_OUT 0
      if (wave == 7) {
        _Pragma("unroll 1") for (int spin = 0; spin < SPIN_CAP; ++spin) {
          const u32x4 q = bld128(rs, WS_FILL + vo);
          if (__all((q[0] == tag) & (q[1] == tag) & (q[2] == tag) & (q[3] == tag))) break;
          __builtin_amdgcn_s_sleep(8);
        }
        ORDER_BEHIND_POLL();
        lds_post(S_FILL, 1);
        if (b == 0 && lane == 0) st4_sc1(ws + WS_EPOCH, tag);
      } else if (wave == 6) {
        _Pragma("unroll 1") for (int spin = 0; spin < SPIN_CAP; ++spin) {
          u32x4 q[8];
#pragma unroll
          for (int i = 0; i < 8; ++i) q[i] = bld128(rs, WS_MOD2 + i * 1024 + vo);
          bool ok = true;
#pragma unroll
          for (int i = 0; i < 8; ++i) ok &= (q[i][0] == tag) & (q[i][2] == tag);
          if (__all(ok)) {
            const u32 kp = (lane >> 2) * 32 + (lane & 1) * 16 + (lane & 2) * 4;
#pragma unroll
            for (int i = 0; i < 2; ++i) {
              lds_w64(M_GA + i * 512 + lane * 8, u32x2{q[i][1], q[i][3]});
              lds_w64(M_SB + i * 512 + kp, u32x2{q[2 + i][1], q[2 + i][3]});
              lds_w64(M_CB + i * 512 + kp, u32x2{q[4 + i][1], q[4 + i][3]});
              lds_w64(M_GB + i * 512 + lane * 8, u32x2{q[6 + i][1], q[6 + i][3]});
            }
            break;
          }
          __builtin_amdgcn_s_sleep(8);
        }
      }
      LP();
      lds_wait<8>(S_AW, AW_GATE);
      STAMP(46);
      LP();
      STAMP(37);
      asm volatile("s_barrier" ::: "memory");
      STAMP(11);
#pragma unroll
      for (int i = 0; i < 16; ++i) lds_w128(T_WO + (wi + i) * 1024 + vo, wot[i]);
      WAITV_BAR(0);
      STAMP(12);
    }
    COLUMN_SUMS();
    STAMP(15);
  }

  const int rot = 2 * (cs & 1);
#define NEWORD 1
#define TILE(TT) (((((TT) & 1) << 1) | ((TT) >> 1)) ^ rot)
  {
  const int lane = fresh_lane(), r = lane & 31, hh = lane >> 5;
  const u32 vo = lane * 16;
#define F1_PRIO 2
#define GATE2 1
#define GATE3 1
#define GATE0 0
#define F1_DELAY 0
  if (F1_PRIO == 1 && wave < 3) __builtin_amdgcn_s_setprio(1);
  if (F1_PRIO == 2 && wave > 3) __builtin_amdgcn_s_setprio(1);
  if (wave == 3) __builtin_amdgcn_s_setprio(2);
  const u32 ysrc = WS_Y2F + 4 * G * 32768;
  u32x4 wdr[28];
  const int cF = tg & 1, cL = 1 - cF;
  const u32 wsrc0 = WS_WDT + (2 * ng * 112 + 14 * wave + 7 * cF) * 1024, wsrc1 = WS_WDT + (2 * ng * 112 + 14 * wave + 7 * cL) * 1024;
#define LOAD_WD(I) wdr[I] = bld128s(rs, vo, ((I) < 14 ? wsrc0 + ((I) >> 1) * 1024 : wsrc1 + (((I) >> 1) - 7) * 1024) + ((I) & 1) * 112 * 1024)
#define WSTG 1
#define H_IN3 7
  u32x4 hb[14];
  const u32 h00 = WS_HF + (2 * tg * 112 + 14 * wave + 7 * cF) * 1024, h01 = WS_HF + (2 * tg * 112 + 14 * wave + 7 * cL) * 1024;
#define LOAD_H0(I) hb[I] = bld128s(rs, vo, ((I) < 7 ? h00 + (I) * 1024 : h01 + ((I) - 7) * 1024))
#define YBOTH 1
#define ST_DELAY 28
  if (wave == 3) {
    if (YBOTH) __builtin_amdgcn_s_sleep(ST_DELAY);
    u32x4 stp[8];
    const u32 stsrc = WS_ST + 2 * G * 4096, stv = hh * 4096 + r * 16;
#pragma unroll
    for (int i = 0; i < 8; ++i) stp[i] = bld128s(rs, stv, stsrc + i * 512);
    if (__builtin_expect(!filled<8>(stp), 0)) {
      _Pragma("unroll 1") for (int spin = 0; spin < SPIN_CAP; ++spin) {
#pragma unroll
        for (int i = 0; i < 8; ++i) stp[i] = bld128s(rs, stv, stsrc + i * 512);
        if (filled<8>(stp)) break;
        __builtin_amdgcn_s_sleep(1);
      }
    }
    STAMP(16);
    {
      float m0 = 0, m1 = 0, q0 = 0, q1 = 0;
#pragma unroll
      for (int i = 0; i < 8; ++i) {
        m0 += __uint_as_float(stp[i][0]); q0 += __uint_as_float(stp[i][1]); m1 += __uint_as_float(stp[i][2]); q1 += __uint_as_float(stp[i][3]);
      }
      m0 *= 1.f / DM; m1 *= 1.f / DM;
      const float v0 = fmaxf(q0 * (1.f / DM) - m0 * m0, 0.f) + 1e-5f, v1 = fmaxf(q1 * (1.f / DM) - m1 * m1, 0.f) + 1e-5f;
      const float r0 = rsqrtf(v0), r1 = rsqrtf(v1), s0 = v0 * r0, s1 = v1 * r1;
      const float mh0 = rbf(-m0), mh1 = rbf(-m1), sh0 = rbf(s0), sh1 = rbf(s1);
      lds_wait<1>(S_STARR, 4);
      lds_w128(T_BX + (64 * hh + 2 * r) * 16, u32x4{pk2(mh0, -m0 - mh0), pk2(mh0, sh0), pk2(s0 - sh0, sh0), 0u});
      lds_w128(T_BX + (64 * hh + 2 * r) * 16 + 16, u32x4{pk2(mh1, -m1 - mh1), pk2(mh1, sh1), pk2(s1 - sh1, sh1), 0u});
      lds_w64(T_RS + (64 * hh + 2 * r) * 4, u32x2{__float_as_uint(r0), __float_as_uint(r1)});
      lds_post(S_STOK, 1);
    }
    STAMP(18);
    _Pragma("unroll 1") for (int spin = 0; spin < SPIN_CAP; ++spin) {
      if (__all(__builtin_amdgcn_raw_buffer_load_b32(rs, (lane & 15) * 4, WS_YF + 2 * G * 32, SC1) == tag)) break;
      __builtin_amdgcn_s_sleep(1);
    }
    ORDER_BEHIND_POLL();
#pragma unroll
    for (int tt = 2; tt < 4; ++tt) {
      const char* src = ws + ysrc + TILE(tt) * 32768;
#pragma unroll
      for (int i = 0; i < 8; ++i) glds4_sc1(src + i * 4096, vo, lb + T_Y + tt * 32768 + i * 4096);
    }
    WAITV(32); lds_post(S_YV, 2);
    WAITV(0);  lds_post(S_YV, 3);
    STAMP(19);
#pragma unroll
    for (int i = 0; i < 28; ++i) LOAD_WD(i);
    auto stage = [&](int s) {
#pragma unroll
      for (int cw = 0; cw < 7; ++cw) {
        const char* src = ws + WS_WDT + (2 * ng * 112 + 14 * (cw < 3 ? cw : cw + 1)) * 1024;
        const u32 dst = lb + T_Y + s * 32768 + cw * 4096;
        if (s < 2) glds4_sc1(src + (7 * cL + 112 * s) * 1024, vo, dst);
        else { glds2_sc1(src + (7 * cF + 4) * 1024, vo, dst); glds2_sc1(src + (7 * cF + 116) * 1024, vo, dst + 2048); }
      }
    };
    lds_wait<2>(S_T0D, 7);
    stage(0);
    lds_wait<2>(S_T1D, 7);
    stage(1);
    WAITV(0); lds_post(S_WS, 2);
    lds_wait<2>(S_T2C, 7);
    stage(2);
    WAITV(0); lds_post(S_WS, 3);
    STAMP(18);
#pragma unroll
    for (int i = 0; i < 14; ++i) LOAD_H0(i);
  } else {
    int wv = wave;
    asm volatile("" : "+s"(wv));
    if (wv >= 4) {
      u32x4 f[16];
      const u32 src0 = ysrc + TILE(0) * 32768 + 8 * (wv & 3) * 1024, src1 = ysrc + TILE(1) * 32768 + 8 * (wv & 3) * 1024;
      __builtin_amdgcn_s_sleep(Y_DELAY2);
#pragma unroll
      for (int i = 0; i < 8; ++i) f[i] = bld128s(rs, vo, src0 + i * 1024);
#pragma unroll
      for (int i = 0; i < 8; ++i) f[8 + i] = bld128s(rs, vo, src1 + i * 1024);
      settle_s<8>(rs, f, vo, src0);
#pragma unroll
      for (int i = 0; i < 8; ++i) lds_w128(T_Y + (8 * (wv & 3) + i) * 1024 + vo, f[i]);
      lds_arrive(S_Y0, lane);
      settle_s<8>(rs, f + 8, vo, src1);
#pragma unroll
      for (int i = 0; i < 8; ++i) lds_w128(T_Y + 32768 + (8 * (wv & 3) + i) * 1024 + vo, f[8 + i]);
      lds_arrive(S_Y1, lane);
    }
    STAMP(16);
    lds_wait<0>(S_Y0, 4);
    STAMP(17);
    PROBE_AT(5);
    if (F1_DELAY > 0 && wave >= 4 && wave < 7) __builtin_amdgcn_s_sleep(F1_DELAY);
#define F1_D 6
#define EPI_PIECE(P, I)                                                                                \
    if constexpr ((I) >= 0 && (I) < 24) {                                                                      \
      constexpr int e_ = (I) / 3, p_ = (I) % 3;                                                        \
      if (p_ == 0) P[e_] = EXP2(rg * P[e_]);                                                           \
      if (p_ == 1) { P[e_] = __builtin_amdgcn_rcpf(1.f + P[e_]); P[8 + e_] *= rsd; }                   \
      if (p_ == 2) { P[e_] *= P[8 + e_]; if (e_ & 1) hv[e_ >> 1] = noempty(pk2(P[e_ - (e_ & 1)], P[e_])); } \
    }
#define EPI_STORE(TT) bst128s(rs, vo, WS_HF + ((4 * G + TILE(TT)) * 112 + at) * 1024, hv)
#define FFN1_STEP(TT, ACC, P, EXTRA)                                                                   \
    {                                                                                                  \
      if (F1_PRIO == 3 && wave != 7) __builtin_amdgcn_s_setprio(3 - (TT));                             \
      if ((TT) == 1) lds_wait<1>(S_Y1, 4);                                                             \
      if ((TT) > 1) lds_wait<1>(S_YV, TT);                                                             \
      const u32 ya = T_Y + (TT) * 32768 + vo;                                                          \
      const u32 ba = T_BX + (TILE(TT) * 32 + r) * 16;                                        \
      u32x4 ring_[F1_D], hv;                                                                           \
      _Pragma("unroll") for (int i = 0; i < F1_D; ++i) ring_[i] = lds_r128(ya + i * 1024);             \
      float rsd = 0, rg = 0;                                                                           \
      if ((TT) > 0) { rsd = lds_rf(T_RS + (TILE(((TT) + 3) & 3) * 32 + r) * 4); rg = -LOG2E * rsd; } \
      const f32x16 zero = {};                                                                          \
      SBAR();                                                                                          \
      static_for<32>([&](auto I_) {                                                                    \
        constexpr int i = I_;                                                                          \
        ACC = MFMA32(w[i], ring_[i % F1_D], i == 0 ? zero : ACC);                                      \
        if (i + F1_D < 32) ring_[i % F1_D] = lds_r128(ya + (i + F1_D) * 1024);                         \
        else if (i + F1_D == 32 && (TT) > 0) ring_[i % F1_D] = lds_r128(ba);                           \
        if ((TT) > 0) { EPI_PIECE(P, i - 2); if (i == 27) EPI_STORE((TT) - 1); }                       \
        EXTRA;                                                                                         \
        SBAR();                                                                                        \
      });                                                                                              \
      if (WSTG && (TT) == 0) lds_arrive(S_T0D, lane);                                                  \
      if (WSTG && (TT) == 2) lds_arrive(S_T2C, lane);                                                  \
      if ((TT) == 0) { lds_wait<1>(S_STOK, 1); ring_[32 % F1_D] = lds_r128(ba); }                      \
      ACC = MFMA32(wX, ring_[32 % F1_D], ACC);                                                         \
      if ((TT) == 1) lds_arrive(S_T1D, lane);                                                          \
      if (GATE2 && (TT) == 3) lds_arrive(S_T2D, lane);                                                 \
      SBAR();                                                                                          \
    }
    f32x16 accA, accB;
#define WD_E 8
#define W3_STEP 2
#define WD_EARLY(TT) if ((i & 7) == 7 && 4 * (TT) + (i >> 3) < WD_E) LOAD_WD(4 * (TT) + (i >> 3))
    FFN1_STEP(0, accA, accB, WD_EARLY(0));
    STAMP(26);
    FFN1_STEP(1, accB, accA, WD_EARLY(1));
    STAMP(27);
    FFN1_STEP(2, accA, accB, WD_EARLY(2));
    STAMP(28);
    static_assert(WD_E == 8, "");
#define W3_AT(I) ((I) == 0 ? 12 : (I) == 1 ? 13 : 20 + (I))
    FFN1_STEP(3, accB, accA, if (i < W3_STEP * 8 && i % W3_STEP == W3_STEP - 1) LOAD_WD(W3_AT(i / W3_STEP)); else if (i >= 32 - H_IN3) LOAD_H0(i - (32 - H_IN3)));
    STAMP(24);
#define H01_EARLY 5
    bool h01_req = false;
    if (H01_EARLY && H_IN3 == 7) {
      h01_req = __builtin_amdgcn_readfirstlane(lds_peek(S_T2D)) >= H01_EARLY;
      if (h01_req) {
#pragma unroll
        for (int i = 7; i < 14; ++i) LOAD_H0(i);
      }
    }
    {
      const float rsd = lds_rf(T_RS + (TILE(3) * 32 + r) * 4), rg = -LOG2E * rsd;
      u32x4 hv;
      static_for<24>([&](auto I_) { constexpr int i = I_; EPI_PIECE(accB, (i % 8) * 3 + i / 8); });
      EPI_STORE(3);
      if (GATE3) lds_arrive(S_T3D, lane);
    }
    {
      const u32 sa_ = T_Y + (wave < 3 ? wave : wave - 1) * 4096 + vo;
#pragma unroll
      for (int i = H_IN3; i < 7; ++i) LOAD_H0(i);
      lds_wait<1>(S_WS, 2);
#pragma unroll
      for (int f = 0; f < 4; ++f) { wdr[14 + 2 * f] = lds_r128(sa_ + f * 1024); wdr[15 + 2 * f] = lds_r128(sa_ + 32768 + f * 1024); }
      lds_wait<1>(S_WS, 3);
      if (!h01_req) {
#pragma unroll
        for (int i = (H_IN3 > 7 ? H_IN3 : 7); i < 14; ++i) LOAD_H0(i);
      }
#pragma unroll
      for (int f = 0; f < 4; ++f) wdr[8 + 2 * (f & 1) + (f >> 1)] = lds_r128(sa_ + 65536 + f * 1024);
      lds_arrive(S_WRD, lane);
    }
  }
  __builtin_amdgcn_s_setprio(0);
  STAMP(20);
  {
    const u32 h10 = h00 + 112 * 1024, h11 = h01 + 112 * 1024;
    static_assert(WSTG, "");
    f32x16 c0 = {}, c1 = {}, d0 = {}, d1 = {};
    settle_s<7>(rs, hb, vo, h00);
    STAMP(21);
#pragma unroll
    for (int i = 0; i < 7; ++i) { const int k = i < 4 ? i : i < 5 ? 6 : i - 1; c0 = MFMA32(wdr[2 * k], hb[k], c0); c1 = MFMA32(wdr[2 * k + 1], hb[k], c1); }
    SBAR();
    if (GATE2) lds_wait<2>(S_T2D, 7);
    __builtin_amdgcn_s_sleep(H_DELAY2);
#pragma unroll
    for (int i = 0; i < 7; ++i) hb[i] = bld128s(rs, vo, h10 + i * 1024);
    SBAR();
    settle_s<7>(rs, hb + 7, vo, h01);
#pragma unroll
    for (int i = 7; i < 14; ++i) { c0 = MFMA32(wdr[2 * i], hb[i], c0); c1 = MFMA32(wdr[2 * i + 1], hb[i], c1); }
    SBAR();
    if (GATE3) lds_wait<2>(S_T3D, 7);
    __builtin_amdgcn_s_sleep(H_DELAY);
#pragma unroll
    for (int i = 0; i < 7; ++i) hb[7 + i] = bld128s(rs, vo, h11 + i * 1024);
    SBAR();
    STAMP(22);
    const int ln = fresh_lane(), r5 = ln & 31, h5 = ln >> 5;
    const u32 ro = T_RED + wave * 8192 + h5 * 512;
#define DUMP(OFF, C0, C1)                                                                              \
    _Pragma("unroll") for (int q = 0; q < 4; ++q) {                                                    \
      lds_wf4(ro + (OFF) + q * 1024 + ((r5 ^ (2 * q + h5)) * 16), f32x4{C0[4 * q], C0[4 * q + 1], C0[4 * q + 2], C0[4 * q + 3]}); \
      lds_wf4(ro + (OFF) + 4096 + q * 1024 + ((r5 ^ (8 + 2 * q + h5)) * 16), f32x4{C1[4 * q], C1[4 * q + 1], C1[4 * q + 2], C1[4 * q + 3]}); \
    }
#define OUT_MODE 0
#define OUT_STORE(H_, P_, A_, B_)                                                                      \
    if (OUT_MODE == 1 || (OUT_MODE == 2 && (H_) == 0)) st8_sc1(P_, A_, B_);                            \
    else if (OUT_MODE == 3) __builtin_nontemporal_store(u32x2{A_, B_}, (u32x2*)(P_));                  \
    else *(u32x2*)(P_) = u32x2{A_, B_}
#define REDUCE(P)                                                                                      \
    {                                                                                                  \
      const int cq = ln & 15, tk = 4 * wave + (ln >> 4);                                               \
      const u32 ra = T_RED + (P) * 65536 + (cq >> 1) * 1024 + (cq & 1) * 512 + ((tk ^ cq) * 16);       \
      f32x4 a = {0, 0, 0, 0};                                                                          \
      _Pragma("unroll") for (int v = 0; v < 8; ++v) a += lds_rf4(ra + v * 8192);                       \
      const u32x2 x2 = lds_r64(T_X2 + (32 * (P) + tk) * 128 + (((cq >> 1) ^ (tk & 7)) * 16) + (cq & 1) * 8); \
      const u32x2 gb = lds_r64(M_GB + (64 * ng + 4 * cq) * 2);                                         \
      OUT_STORE(P, out + (64 * tg + 32 * (P) + tk) * DM + 64 * ng + 4 * cq,                               \
          pk2(lo2f(x2[0]) + lo2f(gb[0]) * a[0], hi2f(x2[0]) + hi2f(gb[0]) * a[1]),                     \
          pk2(lo2f(x2[1]) + lo2f(gb[1]) * a[2], hi2f(x2[1]) + hi2f(gb[1]) * a[3]));                    \
    }
    lds_wait<1>(S_WRD, 7);
    DUMP(0, c0, c1);
    lds_arrive(S_D0, ln);
    settle_s<7>(rs, hb, vo, h10);
#pragma unroll
    for (int i = 0; i < 7; ++i) { d0 = MFMA32(wdr[2 * i], hb[i], d0); d1 = MFMA32(wdr[2 * i + 1], hb[i], d1); }
    SBAR();
    lds_wait<1>(S_D0, 8);
    STAMP(23);
    REDUCE(0);
    SBAR();
    settle_s<7>(rs, hb + 7, vo, h11);
    STAMP(51);
#pragma unroll
    for (int i = 7; i < 14; ++i) { d0 = MFMA32(wdr[2 * i], hb[i], d0); d1 = MFMA32(wdr[2 * i + 1], hb[i], d1); }
    DUMP(65536, d0, d1);
    BAR();
    REDUCE(1);
  }
  }
  STAMPE(25);
}

extern "C" void kernel_launch(void* const* d_in, const int* in_sizes, int n_in, void* d_out, int out_size, void* d_ws, size_t ws_size,
                              hipStream_t stream) {
  (void)in_sizes; (void)n_in; (void)out_size; (void)ws_size;
  (void)hipFuncSetAttribute((const void*)fused_block, hipFuncAttributeMaxDynamicSharedMemorySize, LDS_BYTES);
  fused_block<<<256, 512, LDS_BYTES, stream>>>((const u16*)d_in[0], (const u16*)d_in[1], (const u16*)d_in[2], (const u16*)d_in[3],
                                               (const u16*)d_in[4], (const u16*)d_in[6], (const u16*)d_in[7], (const u16*)d_in[8],
                                               (const u16*)d_in[9], (const u16*)d_in[10], (char*)d_ws, (u16*)d_out);
}
```
